```python
import torch

def _softmax(x, axis=-1):
    return torch.softmax(x, dim=axis)
def _gelu(x):
    return torch.nn.functional.gelu(x, approximate="tanh")
def _silu(x):
    return torch.nn.functional.silu(x)
def _sigmoid(x):
    return torch.sigmoid(x)
def _layernorm(x, axis=-1, eps=1e-5):
    m = x.mean(dim=axis, keepdim=True)
    v = x.var(dim=axis, keepdim=True, unbiased=False)
    return (x - m) / torch.sqrt(v + eps)
def _scatter_add(tgt, idx, src, axis=0):
    return tgt.clone().index_add_(axis, idx, src)
def _segment_sum(src, seg, n):
    out = torch.zeros((n,) + src.shape[1:], dtype=src.dtype)
    return out.index_add_(0, seg, src)
def _pad(x, pw):
    flat = tuple(v for pair in reversed(pw) for v in pair)
    return torch.nn.functional.pad(x, flat)

kernel_name = 'sequential_preLN__grouped_kv_mqa__dense_activation_mish__prompt'

def setup_inputs():

    _w = (lambda a: (lambda t: t.to(torch.bfloat16) if t.is_floating_point() else t)(torch.as_tensor(a)))
    def _A_setup():
        B, S, D, H = 1, 2048, 512, 32
        Dh = D // H
        return {
            "x": _w((lambda *s: torch.randn(*s, dtype=torch.bfloat16) * 0.05)(B, S, D)),
            "wq": _w((lambda *s: torch.randn(*s, dtype=torch.bfloat16) * 0.05)(D, H * Dh)),
            "wk": _w((lambda *s: torch.randn(*s, dtype=torch.bfloat16) * 0.05)(D, Dh)),
            "wv": _w((lambda *s: torch.randn(*s, dtype=torch.bfloat16) * 0.05)(D, Dh)),
            "wo": _w((lambda *s: torch.randn(*s, dtype=torch.bfloat16) * 0.05)(H * Dh, D)),
        }
    _A_in = _A_setup()

    def _B_setup():
        B, S, D, F = 1, 2048, 512, 1792
        return {
            "x": _w((lambda *s: torch.randn(*s, dtype=torch.bfloat16) * 0.05)(B, S, D)),
            "w1": _w((lambda *s: torch.randn(*s, dtype=torch.bfloat16) * 0.8)(D, F) * (D ** -0.5)),
            "b1": _w((lambda *s: torch.randn(*s, dtype=torch.bfloat16) * 0.4)(F)),
            "w2": _w((lambda *s: torch.randn(*s, dtype=torch.bfloat16) * 0.2)(F, D) * (F ** -0.5)),
        }
    _B_in = _B_setup()

    return {
        "A_x": _A_in["x"],
        "A_wq": _A_in["wq"],
        "A_wk": _A_in["wk"],
        "A_wv": _A_in["wv"],
        "A_wo": _A_in["wo"],
        "B_x": _B_in["x"],
        "B_w1": _B_in["w1"],
        "B_b1": _B_in["b1"],
        "B_w2": _B_in["w2"],
    }

def reference(A_x, A_wq, A_wk, A_wv, A_wo, B_x, B_w1, B_b1, B_w2):
    def _A_ref(x, wq, wk, wv, wo):
        B, S, D = x.shape
        H = 32
        Dh = D // H
        q = (lambda x, *d: x.permute(*d))((x @ wq).reshape(B, S, H, Dh), 0, 2, 1, 3)
        k = x @ wk
        v = x @ wv
        scores = torch.einsum("bhqd,bkd->bhqk", q, k) * (Dh ** -0.5)
        p = _softmax(scores, axis=-1)
        o = torch.einsum("bhqk,bkd->bhqd", p, v)
        return (lambda x, *d: x.permute(*d))(o, 0, 2, 1, 3).reshape(B, S, H * Dh) @ wo

    def _B_ref(x, w1, b1, w2):
        h = torch.einsum("bsd,df->bsf", x, w1) + b1
        sp = torch.log1p(torch.exp(-torch.abs(h))) + torch.where(h > 0, h, torch.zeros_like(h))
        act = h * torch.tanh(sp)
        return torch.einsum("bsf,fd->bsd", act, w2)

    x = A_x
    h = x + _A_ref(_layernorm(x, axis=-1), A_wq, A_wk, A_wv, A_wo)
    h2 = h + _B_ref(_layernorm(h, axis=-1), B_w1, B_b1, B_w2)
    return h2
```

```cpp
#include <hip/hip_runtime.h>
#include <hip/hip_bf16.h>

typedef unsigned long long u64;
typedef __attribute__((ext_vector_type(8))) __bf16 bf8;
typedef __attribute__((ext_vector_type(2))) __bf16 bf2;
typedef __attribute__((ext_vector_type(4))) short s4;
typedef __attribute__((ext_vector_type(2))) float f2;
typedef __attribute__((ext_vector_type(4))) float f4;
typedef __attribute__((ext_vector_type(16))) float f16v;
typedef __attribute__((ext_vector_type(2))) unsigned u2;
typedef __attribute__((ext_vector_type(4))) unsigned u4;
#define AS1 __attribute__((address_space(1)))
#define AS3 __attribute__((address_space(3)))
#define DEVI __device__ __forceinline__
#define RLX_AGENT __ATOMIC_RELAXED, __HIP_MEMORY_SCOPE_AGENT

constexpr unsigned WS_E = 0, WS_F1 = 4096, WS_F2 = 8192, WS_F3 = 12288, WS_F4 = 16384, WS_KMAX = 20480;
constexpr unsigned WS_K = 65536, WS_V = 131072, WS_O = 1u << 20, WS_H = 3u << 20, WS_ACT = 5u << 20, WS_END = 16u << 20;
constexpr int L_XN = 0, L_WQ = 65536, L_WKV = 131072;
constexpr int L_K = 0, L_V = 65536;
constexpr int L_WO = 0, L_RED1 = 65536;
constexpr int L_H = 0, L_ST = 131072, L_CS = 132096;
constexpr int L_RED2 = 0;
constexpr int LDS_BYTES = 147456;
constexpr unsigned SPIN_CAP = 400000;
constexpr float LOG2E = 1.4426950408889634f;
constexpr float SAFE_BOUND = 50.0f;

#define NPRE 7
#define W2E 2
#define STAMP(i) do {} while (0)

DEVI void bar() { asm volatile("s_waitcnt lgkmcnt(0)\n\ts_barrier" ::: "memory"); }
DEVI void drain() { asm volatile("s_waitcnt vmcnt(0)" ::: "memory"); }
DEVI unsigned pk(float a, float b) { f2 v = {a, b}; return __builtin_bit_cast(unsigned, __builtin_convertvector(v, bf2)); }
DEVI float lo16(unsigned v) { return __uint_as_float(v << 16); }
DEVI float hi16(unsigned v) { return __uint_as_float(v & 0xffff0000u); }
DEVI float dot2(unsigned a, unsigned b, float c) {
  return __builtin_amdgcn_fdot2_f32_bf16(__builtin_bit_cast(bf2, a), __builtin_bit_cast(bf2, b), c, false);
}
DEVI f16v mfma32(u4 a, u4 b, f16v c) {
  return __builtin_amdgcn_mfma_f32_32x32x16_bf16(__builtin_bit_cast(bf8, a), __builtin_bit_cast(bf8, b), c, 0, 0, 0);
}
DEVI f4 mfma16(u4 a, u4 b, f4 c) {
  return __builtin_amdgcn_mfma_f32_16x16x32_bf16(__builtin_bit_cast(bf8, a), __builtin_bit_cast(bf8, b), c, 0, 0, 0);
}
DEVI u4 lds16(const char* p) { return *(const u4*)p; }
DEVI u2 ldstr(const char* p) { return __builtin_bit_cast(u2, __builtin_amdgcn_ds_read_tr16_b64_v4i16((AS3 s4*)p)); }
DEVI u4 ldstr2(const char* p, int d) { u2 a = ldstr(p), b = ldstr(p + d); return u4{a[0], a[1], b[0], b[1]}; }
DEVI void dma16(const void* g, char* l) { __builtin_amdgcn_global_load_lds((const AS1 void*)g, (AS3 void*)l, 16, 0, 0); }
template <int C> DEVI float dpp(float v) { return __int_as_float(__builtin_amdgcn_update_dpp(0, __float_as_int(v), C, 0xf, 0xf, true)); }
DEVI float swap32_add(float v) { auto r = __builtin_amdgcn_permlane32_swap(__float_as_uint(v), __float_as_uint(v), false, false); return __uint_as_float(r[0]) + __uint_as_float(r[1]); }
DEVI float swap16_add(float v) { auto r = __builtin_amdgcn_permlane16_swap(__float_as_uint(v), __float_as_uint(v), false, false); return __uint_as_float(r[0]) + __uint_as_float(r[1]); }
DEVI float swap32_max(float v) { auto r = __builtin_amdgcn_permlane32_swap(__float_as_uint(v), __float_as_uint(v), false, false); return fmaxf(__uint_as_float(r[0]), __uint_as_float(r[1])); }
DEVI float swap16_max(float v) { auto r = __builtin_amdgcn_permlane16_swap(__float_as_uint(v), __float_as_uint(v), false, false); return fmaxf(__uint_as_float(r[0]), __uint_as_float(r[1])); }
DEVI float max16(float v) {
  v = fmaxf(v, dpp<0xB1>(v)); v = fmaxf(v, dpp<0x4E>(v)); v = fmaxf(v, dpp<0x141>(v)); return fmaxf(v, dpp<0x140>(v));
}
DEVI void put_flag(unsigned char* ws, unsigned off, u64 tag) { __hip_atomic_store((AS1 u64*)(ws + off), tag, RLX_AGENT); }
template <int N> DEVI void wait_flags(unsigned char* ws, unsigned off, u64 tag) {
  for (unsigned spins = 0; spins < SPIN_CAP; ++spins) {
    bool ok = true;
#pragma unroll
    for (int k = 0; k < N; ++k) ok &= __hip_atomic_load((AS1 u64*)(ws + off + k * 512), RLX_AGENT) == tag;
    if (__all(ok)) break;
    __builtin_amdgcn_s_sleep(1);
  }
  __builtin_amdgcn_fence(__ATOMIC_ACQUIRE, "wavefront");
  asm volatile("" ::: "memory");
}
#define LD_SC1(so, vo) __builtin_amdgcn_raw_buffer_load_b128(rs, (vo), (so), 16)
#define ST_SC1(v, so, vo) __builtin_amdgcn_raw_buffer_store_b128((v), rs, (vo) + (so), 0, 16)

DEVI void pv_tile(const f16v& p, u4 v0, u4 v1, f16v& o) {
  u4 p0 = {pk(p[0], p[1]), pk(p[2], p[3]), pk(p[4], p[5]), pk(p[6], p[7])};
  o = mfma32(v0, p0, o);
  u4 p1 = {pk(p[8], p[9]), pk(p[10], p[11]), pk(p[12], p[13]), pk(p[14], p[15])};
  o = mfma32(v1, p1, o);
}
DEVI void tile_slow(const char* kp, const char* vp, u4 q, float& m, f16v& o) {
  u4 v0 = lds16(vp), v1 = lds16(vp + 544);
  f16v z = {0};
  f16v s = mfma32(lds16(kp), q, z);
  float t = s[0];
#pragma unroll
  for (int i = 1; i < 16; ++i) t = fmaxf(t, s[i]);
  float mn = fmaxf(m, swap32_max(t));
  float al = __builtin_amdgcn_exp2f(m - mn);
  m = mn;
  f16v p;
#pragma unroll
  for (int i = 0; i < 16; ++i) { p[i] = __builtin_amdgcn_exp2f(s[i] - mn); o[i] *= al; }
  pv_tile(p, v0, v1, o);
}

__global__ __launch_bounds__(512, 2) void fused_block_kernel(
    const unsigned char* __restrict__ x, const unsigned char* __restrict__ wq, const unsigned char* __restrict__ wk,
    const unsigned char* __restrict__ wv, const unsigned char* __restrict__ wo, const unsigned char* __restrict__ w1,
    const unsigned char* __restrict__ b1, const unsigned char* __restrict__ w2, unsigned char* __restrict__ out,
    unsigned char* ws) {
  __shared__ __attribute__((aligned(16))) char smem[LDS_BYTES];
  const int tid = threadIdx.x, lane = tid & 63;
  const int w = __builtin_amdgcn_readfirstlane(tid >> 6);
  const int bid = blockIdx.x;
  const int rb = (bid & 7) * 4 + ((bid >> 3) & 3), j = bid >> 5;
  const int R0 = rb * 64;
  const int c16 = lane & 15, g4 = lane >> 4, r32 = lane & 31, h2 = lane >> 5;
  const __amdgpu_buffer_rsrc_t rs = __builtin_amdgcn_make_buffer_rsrc((void*)ws, 0, (int)WS_END, 0x00020000);
  STAMP(0);
  const u64 e0 = __hip_atomic_load((AS1 u64*)(ws + WS_E), RLX_AGENT);

  {
    const unsigned char* xr = x + (size_t)(R0 + 8 * w + (lane >> 3)) * 1024 + (lane & 7) * 16;
    u4 xv[8];
#pragma unroll
    for (int i = 0; i < 8; ++i) xv[i] = *(const u4*)(xr + i * 128);
    const unsigned char* wkv = (j & 1) ? wv : wk;
#pragma unroll
    for (int i = 0; i < 2; ++i) dma16(wkv + (size_t)((w * 2 + i) * 64 + lane) * 16, smem + L_WKV + (w * 2 + i) * 1024);
#pragma unroll
    for (int i = 0; i < 8; ++i) {
      const int k = i * 64 + w * 8 + (lane >> 3), pc = lane & 7;
      const int f = ((lane >> 4) & 1) | ((w & 1) << 1);
      dma16(wq + (size_t)k * 1024 + j * 128 + ((((pc >> 1) ^ f) << 1) | (pc & 1)) * 16, smem + L_WQ + (i * 8 + w) * 1024);
    }
    float s1 = 0.f, s2 = 0.f;
#pragma unroll
    for (int i = 0; i < 8; ++i)
#pragma unroll
      for (int e = 0; e < 4; ++e) { s1 = dot2(xv[i][e], 0x3f803f80u, s1); s2 = dot2(xv[i][e], xv[i][e], s2); }
    s1 += dpp<0xB1>(s1); s2 += dpp<0xB1>(s2);
    s1 += dpp<0x4E>(s1); s2 += dpp<0x4E>(s2);
    s1 += dpp<0x141>(s1); s2 += dpp<0x141>(s2);
    const float mean = s1 * (1.f / 512.f);
    const float rstd = __builtin_amdgcn_rsqf(fmaxf(s2 * (1.f / 512.f) - mean * mean, 0.f) + 1e-5f);
    const float nm = -mean * rstd;
    const int rl = 8 * w + (lane >> 3), q7 = lane & 7;
    char* xw = smem + L_XN + (((rl >> 4) * 16 + (q7 >> 2)) * 64 + (q7 & 3) * 16 + ((rl & 15) ^ q7)) * 16;
#pragma unroll
    for (int i = 0; i < 8; ++i) {
      u4 y;
#pragma unroll
      for (int e = 0; e < 4; ++e) y[e] = pk(fmaf(lo16(xv[i][e]), rstd, nm), fmaf(hi16(xv[i][e]), rstd, nm));
      *(u4*)(xw + i * 2048) = y;
    }
  }
  const u64 tag = (((u64)__builtin_amdgcn_readfirstlane((unsigned)(e0 >> 32)) << 32) | (unsigned)__builtin_amdgcn_readfirstlane((unsigned)e0)) + 1;
  STAMP(1);
  drain();
  bar();
  STAMP(2);
  const int xo0 = (g4 * 16 + (c16 ^ g4)) * 16, xo1 = (g4 * 16 + (c16 ^ (4 + g4))) * 16;
  if (w == 0) {
    const char* wp = smem + L_WKV + (8 * g4 + (c16 >> 2)) * 32 + (c16 & 3) * 8;
    const char* xp = smem + L_XN + (j >> 1) * 16384;
    f4 a0 = {0}, a1 = {0};
#pragma unroll
    for (int s = 0; s < 16; s += 2) {
      u4 w0 = ldstr2(wp + s * 1024, 128), w1f = ldstr2(wp + (s + 1) * 1024, 128);
      u4 x0 = lds16(xp + s * 1024 + xo0), x1 = lds16(xp + (s + 1) * 1024 + xo1);
      if (j & 1) { a0 = mfma16(x0, w0, a0); a1 = mfma16(x1, w1f, a1); }
      else       { a0 = mfma16(w0, x0, a0); a1 = mfma16(w1f, x1, a1); }
    }
    a0 += a1;
    u2 kv = {pk(a0[0], a0[1]), pk(a0[2], a0[3])};
    const int key0 = R0 + 16 * (j >> 1);
    if (j & 1) {
      __builtin_amdgcn_raw_buffer_store_b64(kv, rs, WS_V + (key0 >> 4) * 512 + (g4 & 1) * 256 + c16 * 16 + (g4 >> 1) * 8, 0, 16);
    } else {
      __builtin_amdgcn_raw_buffer_store_b64(kv, rs, WS_K + (key0 >> 5) * 1024 + ((g4 >> 1) * 32 + (key0 & 16) + c16) * 16 + (g4 & 1) * 8, 0, 16);
      float n2 = dot2(kv[0], kv[0], dot2(kv[1], kv[1], 0.f));
      n2 = max16(swap32_add(swap16_add(n2)));
      if (lane == 0) __builtin_amdgcn_raw_buffer_store_b32(__float_as_uint(n2), rs, WS_KMAX + (rb * 4 + (j >> 1)) * 4, 0, 16);
    }
    drain();
    if (lane == 0) put_flag(ws, WS_F1 + (rb * 8 + j) * 8, tag);
  }
  STAMP(3);
  const int rt = w & 1, ha = w >> 1;
  u4 qf;
  {
    const int f = ((c16 >> 3) & 1) | ((g4 & 1) << 1);
    const char* wp = smem + L_WQ + (8 * g4 + (c16 >> 2)) * 128 + ((ha ^ f) * 2) * 16 + (c16 & 3) * 8;
    const char* xp = smem + L_XN + rt * 32768;
    f4 a0 = {0}, a1 = {0};
#pragma unroll
    for (int s = 0; s < 16; ++s) {
      u4 wf = ldstr2(wp + s * 4096, 512);
      const int xo = (s & 1) ? xo1 : xo0;
      a0 = mfma16(wf, lds16(xp + s * 1024 + xo), a0);
      a1 = mfma16(wf, lds16(xp + 16384 + s * 1024 + xo), a1);
    }
    const float qs = 0.25f * LOG2E;
    auto r0 = __builtin_amdgcn_permlane16_swap(pk(a0[0] * qs, a0[1] * qs), pk(a1[0] * qs, a1[1] * qs), false, false);
    auto r1 = __builtin_amdgcn_permlane16_swap(pk(a0[2] * qs, a0[3] * qs), pk(a1[2] * qs, a1[3] * qs), false, false);
    qf = u4{r0[0], r1[0], r0[1], r1[1]};
  }
  STAMP(4);
  if (w == 0) wait_flags<4>(ws, WS_F1 + lane * 8, tag);
  bar();
  STAMP(5);

  unsigned w1r[16][8];
  const __amdgpu_buffer_rsrc_t rw1 = __builtin_amdgcn_make_buffer_rsrc((void*)w1, 0, 512 * 3584, 0x00020000);
  const int w1o = (16 * (g4 >> 1) + 4 * (g4 & 1)) * 3584 + (224 * j + 32 * (w < 7 ? w : 6) + 2 * c16) * 2;
#define W1_LOAD1(s, e) w1r[s][e] = __builtin_amdgcn_raw_buffer_load_b32(rw1, w1o, (32 * (s) + 8 * ((e) >> 2) + ((e) & 3)) * 3584, 0)
#define W1_LOAD(s) _Pragma("unroll") for (int e = 0; e < 8; ++e) W1_LOAD1(s, e)
  f16v o = {0};
  u2 xres0, xres1;
  {
    u4 st[8][2];
#define KV_LOAD(i) do { st[i][0] = LD_SC1(WS_K + (i) * 8192, tid * 16); st[i][1] = LD_SC1(WS_V + (i) * 8192, tid * 16); } while (0)
#define KV_PUT(i) do { *(u4*)(kw + (i) * 8192) = st[i][0]; *(u4*)(vw + (i) * 8704) = st[i][1]; } while (0)
    KV_LOAD(0);
    const u2 km = __builtin_bit_cast(u2, __builtin_amdgcn_raw_buffer_load_b64(rs, WS_KMAX + lane * 8, 0, 16));
    if (tid < 256) *(u4*)(smem + L_V + (tid >> 1) * 544 + (tid & 1) * 272 + 256) = u4{0x3f803f80u, 0x3f803f80u, 0x3f803f80u, 0x3f803f80u};
    char* kw = smem + L_K + tid * 16;
    char* vw = smem + L_V + (tid >> 5) * 544 + ((tid >> 4) & 1) * 272 + (tid & 15) * 16;
    float qn = swap32_add(dot2(qf[0], qf[0], dot2(qf[1], qf[1], dot2(qf[2], qf[2], dot2(qf[3], qf[3], 0.f)))));
    KV_PUT(0);
    float kn = fmaxf(__uint_as_float(km[0]), __uint_as_float(km[1]));
    kn = swap32_max(swap16_max(max16(kn)));
    const float bound = __builtin_sqrtf(qn * kn) * 1.001f;
    const bool safe = __all(bound <= SAFE_BOUND);
    f16v cneg;
#pragma unroll
    for (int i = 0; i < 16; ++i) cneg[i] = -bound;
    bar();
    STAMP(6);
    const char* kp = smem + L_K + lane * 16;
    const char* vp = smem + L_V + h2 * 272 + (r32 < 16 ? r32 : ((r32 == 16 || r32 == 20) ? 16 : 0)) * 16;
    unsigned va = (unsigned)(size_t)(const AS3 char*)vp, vb = va + 32 * 1088;
    asm volatile("" : "+v"(va), "+v"(vb));
#define V_READ(t, hf) (*(const AS3 u4*)(size_t)(((t) < 32 ? va + (t) * 1088 : vb + ((t) - 32) * 1088) + (hf) * 544))
#define SB() __builtin_amdgcn_sched_barrier(0)
#define EXPS(a, b) _Pragma("unroll") for (int i = a; i < b; ++i) s[i] = __builtin_amdgcn_exp2f(s[i])
    unsigned pf0 = 0, pf1 = 0;
    f16v s = mfma32(lds16(kp), qf, cneg);
    u4 kf = lds16(kp + 1024), v0 = V_READ(0, 0), v1, p1;
#pragma unroll
    for (int t = 0; t < 64; ++t) {
      SB();
      if (t == 6 || t == 16) bar();
      u4 kfn, v0n;
      if (t < 62) kfn = lds16(kp + (t + 2) * 1024);
      if (t < 63) v0n = V_READ(t + 1, 0);
      if (t < 7) KV_LOAD(t + 1);
      if (t >= 2 && t < 9) KV_PUT(t - 1);
      if (t >= 8 && t < 8 + 8 * NPRE) {
        const int sx = (t - 8) >> 2, e = 2 * ((t - 8) & 3);
        W1_LOAD1(sx, e); W1_LOAD1(sx, e + 1);
      }
      if (t == 7) {
        const unsigned char* xr = x + (size_t)(R0 + 32 * rt + r32) * 1024 + (64 * j + 16 * ha + 4 * h2) * 2;
        xres0 = *(const u2*)xr; xres1 = *(const u2*)(xr + 16);
        const int cl = (bid >> 3) & 3;
        pf0 = *(const unsigned*)((w < 7 ? w2 + (size_t)(cl * 448 + w * 64 + lane) * 1024
                                        : wo + (size_t)(cl * 128 + lane) * 1024) + j * 128);
        pf1 = *(const unsigned*)(wo + (size_t)(cl * 128 + 64 + lane) * 1024 + j * 128);
      }
      if (t == 24) asm volatile("" :: "v"(pf0), "v"(pf1));
      SB();
      if (t > 0) o = mfma32(v1, p1, o);
      SB();
      v1 = V_READ(t, 1);
      EXPS(0, 4);
      SB();
      f16v sn;
      if (t < 63) sn = mfma32(kf, qf, cneg);
      SB();
      EXPS(4, 8);
      SB();
      const u4 p0 = {pk(s[0], s[1]), pk(s[2], s[3]), pk(s[4], s[5]), pk(s[6], s[7])};
      SB();
      EXPS(8, 10);
      SB();
      o = mfma32(v0, p0, o);
      SB();
      EXPS(10, 16);
      SB();
      p1 = u4{pk(s[8], s[9]), pk(s[10], s[11]), pk(s[12], s[13]), pk(s[14], s[15])};
      s = sn; kf = kfn; v0 = v0n;
    }
    o = mfma32(v1, p1, o);
    SB();
    if (!safe) {
      float m = -3.0e38f;
      o = f16v{0};
#pragma unroll 1
      for (int t = 0; t < 64; ++t) tile_slow(kp + t * 1024, vp + t * 1088, qf, m, o);
    }
  }
  STAMP(7);
  {
    const float il = __builtin_amdgcn_rcpf(o[8]);
    u4 of = {pk(o[0] * il, o[1] * il), pk(o[2] * il, o[3] * il), pk(o[4] * il, o[5] * il), pk(o[6] * il, o[7] * il)};
    ST_SC1(of, WS_O + ((rb * 2 + rt) * 32 + 4 * j + ha) * 1024, lane * 16);
  }
  drain();
  bar();
  if (tid == 0) put_flag(ws, WS_F2 + (rb * 8 + j) * 8, tag);
  STAMP(8);

#pragma unroll
  for (int i = 0; i < 8; ++i) {
    const int k = i * 64 + w * 8 + (lane >> 3), pc = lane & 7;
    dma16(wo + (size_t)k * 1024 + j * 128 + (pc ^ (((lane >> 4) & 1) << 2)) * 16, smem + L_WO + (i * 8 + w) * 1024);
  }
#pragma unroll
  for (int s = 2 * NPRE; s < 16; ++s) W1_LOAD(s);
  u4 a1f[16][2];
  f4 cs0 = {0}, cs1 = {0};
#define W1_PERM(s) _Pragma("unroll") for (int e = 0; e < 4; ++e) { \
    a1f[s][0][e] = __builtin_amdgcn_perm(w1r[s][2 * e + 1], w1r[s][2 * e], 0x05040100u); \
    a1f[s][1][e] = __builtin_amdgcn_perm(w1r[s][2 * e + 1], w1r[s][2 * e], 0x07060302u); } \
    cs0 = mfma16(a1f[s][0], ones, cs0); cs1 = mfma16(a1f[s][1], ones, cs1)
  const u4 ones = {0x3f803f80u, 0x3f803f80u, 0x3f803f80u, 0x3f803f80u};
#pragma unroll
  for (int s = 0; s < 2 * NPRE; ++s) { W1_PERM(s); }
  const int kq = ha;
  wait_flags<1>(ws, WS_F2 + (rb * 8 + 2 * kq + (lane & 1)) * 8, tag);
  STAMP(9);
  float hv[8];
  {
    u4 ob[8];
#pragma unroll
    for (int i = 0; i < 8; ++i) ob[i] = LD_SC1(WS_O + ((rb * 2 + rt) * 32 + 8 * kq + i) * 1024, lane * 16);
    asm volatile("s_waitcnt vmcnt(8)" ::: "memory");
    bar();
    const int q = c16 >> 2, u = g4 & 1;
    const char* wp = smem + L_WO + (kq * 128 + 4 * h2 + q) * 128 + (c16 & 3) * 8;
    const int po0 = (u ^ ((q >> 1) << 1)) * 32, po1 = ((2 + u) ^ ((q >> 1) << 1)) * 32;
    f16v acc[2] = {{0}, {0}};
#pragma unroll
    for (int i = 0; i < 8; ++i) {
      acc[0] = mfma32(ldstr2(wp + i * 2048 + po0, 1024), ob[i], acc[0]);
      acc[1] = mfma32(ldstr2(wp + i * 2048 + po1, 1024), ob[i], acc[1]);
    }
    STAMP(10);
#pragma unroll
    for (int d = 0; d < 4; ++d) {
      if (d != kq) {
        char* rp = smem + L_RED1 + ((rt + 2 * d) * 4 + kq) * 2048 + lane * 16;
        const f16v& a = acc[d >> 1];
        const int b = 8 * (d & 1);
        *(f4*)rp = f4{a[b], a[b + 1], a[b + 2], a[b + 3]};
        *(f4*)(rp + 1024) = f4{a[b + 4], a[b + 5], a[b + 6], a[b + 7]};
      } else {
#pragma unroll
        for (int e = 0; e < 8; ++e) hv[e] = acc[d >> 1][8 * (d & 1) + e];
      }
    }
    bar();
#pragma unroll
    for (int d = 1; d < 4; ++d) {
      const char* rp = smem + L_RED1 + (w * 4 + ((kq + d) & 3)) * 2048 + lane * 16;
      f4 a = *(const f4*)rp, b = *(const f4*)(rp + 1024);
#pragma unroll
      for (int e = 0; e < 4; ++e) { hv[e] += a[e]; hv[4 + e] += b[e]; }
    }
    u4 hf = {pk(hv[0] + lo16(xres0[0]), hv[1] + hi16(xres0[0])), pk(hv[2] + lo16(xres0[1]), hv[3] + hi16(xres0[1])),
             pk(hv[4] + lo16(xres1[0]), hv[5] + hi16(xres1[0])), pk(hv[6] + lo16(xres1[1]), hv[7] + hi16(xres1[1]))};
    ST_SC1(hf, WS_H + ((rb * 2 + rt) * 32 + 4 * j + kq) * 1024, lane * 16);
  }
  drain();
  bar();
  if (tid == 0) put_flag(ws, WS_F3 + (rb * 8 + j) * 8, tag);
  STAMP(11);

  {
    const u4 b1v = *(const u4*)(b1 + (224 * j + 32 * (w < 7 ? w : 6) + 8 * g4) * 2);
#pragma unroll
    for (int s = 2 * NPRE; s < 16; ++s) { W1_PERM(s); }
    if (c16 == 0) {
      char* cp = smem + L_CS + (w * 4 + g4) * 64;
      *(f4*)cp = cs0; *(f4*)(cp + 16) = cs1;
      *(f4*)(cp + 32) = f4{lo16(b1v[0]), lo16(b1v[1]), lo16(b1v[2]), lo16(b1v[3])};
      *(f4*)(cp + 48) = f4{hi16(b1v[0]), hi16(b1v[1]), hi16(b1v[2]), hi16(b1v[3])};
    }
  }
  u2 w2r[7][8];
  const __amdgpu_buffer_rsrc_t rw2 = __builtin_amdgcn_make_buffer_rsrc((void*)w2, 0, 1792 * 1024, 0x00020000);
  const int w2o = (224 * w + 8 * g4) * 1024 + (64 * j + 4 * c16) * 2;
#define W2_LOAD(s) _Pragma("unroll") for (int e = 0; e < 8; ++e) w2r[s][e] = __builtin_bit_cast(u2, __builtin_amdgcn_raw_buffer_load_b64(rw2, w2o + (e & 3) * 1024, (32 * (s) + (e & 4)) * 1024, 0))
#pragma unroll
  for (int s = 0; s < W2E; ++s) { W2_LOAD(s); }
  wait_flags<1>(ws, WS_F3 + (rb * 8 + w) * 8, tag);
  STAMP(12);
  {
    u4 hb[8];
#pragma unroll
    for (int i = 0; i < 8; ++i) hb[i] = LD_SC1(WS_H + ((rb * 2 + (i >> 2)) * 32 + 4 * w + (i & 3)) * 1024, lane * 16);
#pragma unroll
    for (int i = 0; i < 8; ++i) *(u4*)(smem + L_H + ((i >> 2) * 32 + 4 * w + (i & 3)) * 1024 + lane * 16) = hb[i];
  }
  bar();
  STAMP(13);
  u2 hr0, hr1;
  {
    const int mt = w >> 1;
    const char* hp = smem + L_H + ((mt >> 1) * 32 + 4 * j + g4) * 1024 + (16 * (mt & 1) + c16) * 16 + (w & 1) * 8;
    hr0 = *(const u2*)hp; hr1 = *(const u2*)(hp + 512);
  }
  {
    const char* hp = smem + L_H + (g4 >> 1) * 1024 + ((g4 & 1) * 32 + c16) * 16;
#define H_READ(s, mt) lds16(hp + ((mt) >> 1) * 32768 + (s) * 2048 + ((mt) & 1) * 256)
    if (w == 7) {
      float s1[4] = {}, s2[4] = {};
#pragma unroll 2
      for (int s = 0; s < 16; ++s)
#pragma unroll
        for (int mt = 0; mt < 4; ++mt) {
          const u4 v = H_READ(s, mt);
#pragma unroll
          for (int e = 0; e < 4; ++e) { s1[mt] = dot2(v[e], 0x3f803f80u, s1[mt]); s2[mt] = dot2(v[e], v[e], s2[mt]); }
        }
#pragma unroll
      for (int mt = 0; mt < 4; ++mt) {
        const float mean = swap32_add(swap16_add(s1[mt])) * (1.f / 512.f);
        const float rstd = __builtin_amdgcn_rsqf(fmaxf(swap32_add(swap16_add(s2[mt])) * (1.f / 512.f) - mean * mean, 0.f) + 1e-5f);
        if (g4 == 0) *(f2*)(smem + L_ST + (16 * mt + c16) * 8) = f2{mean, rstd};
      }
      bar();
    } else {
      f4 acc[2][4] = {};
      u4 hb[2][4];
#pragma unroll
      for (int mt = 0; mt < 4; ++mt) hb[0][mt] = H_READ(0, mt);
#pragma unroll
      for (int s = 0; s < 16; ++s) {
        if (s < 15) {
#pragma unroll
          for (int mt = 0; mt < 4; ++mt) hb[(s + 1) & 1][mt] = H_READ(s + 1, mt);
        }
        SB();
#pragma unroll
        for (int mt = 0; mt < 4; ++mt) {
          acc[0][mt] = mfma16(a1f[s][0], hb[s & 1][mt], acc[0][mt]);
          acc[1][mt] = mfma16(a1f[s][1], hb[s & 1][mt], acc[1][mt]);
        }
        SB();
      }
      STAMP(14);
      bar();
      const char* cp = smem + L_CS + (w * 4 + g4) * 64;
      const f4 c0 = *(const f4*)cp, c1 = *(const f4*)(cp + 16), bb0 = *(const f4*)(cp + 32), bb1 = *(const f4*)(cp + 48);
#pragma unroll
      for (int mt = 0; mt < 4; ++mt) {
        const f2 st = *(const f2*)(smem + L_ST + (16 * mt + c16) * 8);
        float y[8];
#pragma unroll
        for (int e = 0; e < 8; ++e) {
          const float cs = (e & 1) ? c1[e >> 1] : c0[e >> 1];
          const float bb = (e & 1) ? bb1[e >> 1] : bb0[e >> 1];
          const float v = fmaf(st[1], fmaf(-st[0], cs, acc[e & 1][mt][e >> 1]), bb);
          const float ex = __builtin_amdgcn_exp2f(v * LOG2E);
          const float r = __builtin_amdgcn_rcpf(fmaf(ex, ex + 2.f, 2.f));
          y[e] = fmaf(-2.f, v * r, v);
        }
        ST_SC1((u4{pk(y[0], y[1]), pk(y[2], y[3]), pk(y[4], y[5]), pk(y[6], y[7])}), WS_ACT + ((rb * 4 + mt) * 56 + 7 * j + w) * 1024, lane * 16);
      }
    }
  }
  drain();
  bar();
  if (tid == 0) put_flag(ws, WS_F4 + (rb * 8 + j) * 8, tag);
  STAMP(15);
#pragma unroll
  for (int s = W2E; s < 7; ++s) { W2_LOAD(s); }

  u4 a2f[7][4];
#pragma unroll
  for (int s = 0; s < 7; ++s)
#pragma unroll
    for (int t = 0; t < 4; ++t)
#pragma unroll
      for (int e = 0; e < 4; ++e)
        a2f[s][t][e] = __builtin_amdgcn_perm(w2r[s][2 * e + 1][t >> 1], w2r[s][2 * e][t >> 1], (t & 1) ? 0x07060302u : 0x05040100u);
  wait_flags<1>(ws, WS_F4 + (rb * 8 + w) * 8, tag);
  STAMP(16);
  {
    f4 acc[4][4] = {};
    u4 ab[7][4];
#define ACT_LOAD(s) _Pragma("unroll") for (int mt = 0; mt < 4; ++mt) ab[s][mt] = LD_SC1(WS_ACT + ((rb * 4 + mt) * 56 + 7 * w + (s)) * 1024, lane * 16)
    ACT_LOAD(0); ACT_LOAD(1); ACT_LOAD(2);
#pragma unroll
    for (int s = 0; s < 7; ++s) {
#pragma unroll
      for (int mt = 0; mt < 4; ++mt)
#pragma unroll
        for (int t = 0; t < 4; ++t) acc[t][mt] = mfma16(a2f[s][t], ab[s][mt], acc[t][mt]);
      if (s + 3 < 7) { ACT_LOAD(s + 3); }
      __builtin_amdgcn_sched_barrier(0);
    }
    STAMP(17);
    float y[8];
#pragma unroll
    for (int d = 0; d < 8; ++d) {
      const int mt = d >> 1, b = 2 * (d & 1);
      if (d != w) {
        char* rp = smem + L_RED2 + (d * 8 + w) * 2048 + lane * 16;
        *(f4*)rp = f4{acc[0][mt][b], acc[1][mt][b], acc[2][mt][b], acc[3][mt][b]};
        *(f4*)(rp + 1024) = f4{acc[0][mt][b + 1], acc[1][mt][b + 1], acc[2][mt][b + 1], acc[3][mt][b + 1]};
      } else {
#pragma unroll
        for (int e = 0; e < 8; ++e) y[e] = acc[e & 3][mt][b + (e >> 2)];
      }
    }
    bar();
#pragma unroll
    for (int d = 1; d < 8; ++d) {
      const char* rp = smem + L_RED2 + (w * 8 + ((w + d) & 7)) * 2048 + lane * 16;
      f4 a = *(const f4*)rp, b = *(const f4*)(rp + 1024);
#pragma unroll
      for (int e = 0; e < 4; ++e) { y[e] += a[e]; y[4 + e] += b[e]; }
    }
    u4 r = {pk(y[0] + lo16(hr0[0]), y[1] + hi16(hr0[0])), pk(y[2] + lo16(hr0[1]), y[3] + hi16(hr0[1])),
            pk(y[4] + lo16(hr1[0]), y[5] + hi16(hr1[0])), pk(y[6] + lo16(hr1[1]), y[7] + hi16(hr1[1]))};
    *(u4*)(out + (size_t)(R0 + 16 * (w >> 1) + c16) * 1024 + (64 * j + 16 * g4 + 8 * (w & 1)) * 2) = r;
  }
  if (bid == 0 && tid == 0) put_flag(ws, WS_E, tag);
  STAMP(18);
}

extern "C" void kernel_launch(void* const* d_in, const int* in_sizes, int n_in, void* d_out, int out_size, void* d_ws,
                              size_t ws_size, hipStream_t stream) {
  typedef const unsigned char* cp;
  fused_block_kernel<<<256, 512, 0, stream>>>((cp)d_in[0], (cp)d_in[1], (cp)d_in[2], (cp)d_in[3], (cp)d_in[4], (cp)d_in[6],
                                              (cp)d_in[7], (cp)d_in[8], (unsigned char*)d_out, (unsigned char*)d_ws);
}
```

```cpp
#include <hip/hip_runtime.h>
#include <hip/hip_bf16.h>

typedef unsigned long long u64;
typedef __attribute__((ext_vector_type(8))) __bf16 bf8;
typedef __attribute__((ext_vector_type(2))) __bf16 bf2;
typedef __attribute__((ext_vector_type(4))) short s4;
typedef __attribute__((ext_vector_type(2))) float f2;
typedef __attribute__((ext_vector_type(4))) float f4;
typedef __attribute__((ext_vector_type(16))) float f16v;
typedef __attribute__((ext_vector_type(2))) unsigned u2;
typedef __attribute__((ext_vector_type(4))) unsigned u4;
#define AS1 __attribute__((address_space(1)))
#define AS3 __attribute__((address_space(3)))
#define DEVI __device__ __forceinline__
#define RLX_AGENT __ATOMIC_RELAXED, __HIP_MEMORY_SCOPE_AGENT

constexpr unsigned WS_E = 0, WS_F1 = 4096, WS_KMAX = 20480;
constexpr unsigned WS_F2 = 0x30000, WS_F3 = 0x38000, WS_F4 = 0x40000;
constexpr unsigned WS_K = 65536, WS_V = 131072, WS_O = 1u << 20, WS_H = 3u << 20, WS_ACT = 5u << 20, WS_END = 16u << 20;
constexpr int L_XN = 0, L_WQ = 65536, L_WKV = 131072, L_QX = 131072;
constexpr int L_K = 0, L_V = 65536, L_OX = 131072;
constexpr int L_WO = 0, L_RED1 = 65536;
constexpr int L_H = 0, L_W2A = 65536, L_ST = 131072, L_CS = 132096;
constexpr int L_RED2 = 0;
constexpr int LDS_BYTES = 163840;
constexpr unsigned SPIN_CAP = 400000;
constexpr float LOG2E = 1.4426950408889634f;
constexpr float SAFE_BOUND = 50.0f;

#define KVS 5
#define PSL 2
#define PSL1 4
#define STAMP(i) do {} while (0)

DEVI void bar() { asm volatile("s_waitcnt lgkmcnt(0)\n\ts_barrier" ::: "memory"); }
DEVI void drain() { asm volatile("s_waitcnt vmcnt(0)" ::: "memory"); }
DEVI unsigned pk(float a, float b) { f2 v = {a, b}; return __builtin_bit_cast(unsigned, __builtin_convertvector(v, bf2)); }
DEVI float lo16(unsigned v) { return __uint_as_float(v << 16); }
DEVI float hi16(unsigned v) { return __uint_as_float(v & 0xffff0000u); }
DEVI float dot2(unsigned a, unsigned b, float c) {
  return __builtin_amdgcn_fdot2_f32_bf16(__builtin_bit_cast(bf2, a), __builtin_bit_cast(bf2, b), c, false);
}
DEVI f16v mfma32(u4 a, u4 b, f16v c) {
  return __builtin_amdgcn_mfma_f32_32x32x16_bf16(__builtin_bit_cast(bf8, a), __builtin_bit_cast(bf8, b), c, 0, 0, 0);
}
DEVI f4 mfma16(u4 a, u4 b, f4 c) {
  return __builtin_amdgcn_mfma_f32_16x16x32_bf16(__builtin_bit_cast(bf8, a), __builtin_bit_cast(bf8, b), c, 0, 0, 0);
}
DEVI u4 lds16(const char* p) { return *(const u4*)p; }
DEVI u2 ldstr(const char* p) { return __builtin_bit_cast(u2, __builtin_amdgcn_ds_read_tr16_b64_v4i16((AS3 s4*)p)); }
DEVI u4 ldstr2(const char* p, int d) { u2 a = ldstr(p), b = ldstr(p + d); return u4{a[0], a[1], b[0], b[1]}; }
DEVI void dma16(const void* g, char* l) { __builtin_amdgcn_global_load_lds((const AS1 void*)g, (AS3 void*)l, 16, 0, 0); }
template <int C> DEVI float dpp(float v) { return __int_as_float(__builtin_amdgcn_update_dpp(0, __float_as_int(v), C, 0xf, 0xf, true)); }
DEVI float swap32_add(float v) { auto r = __builtin_amdgcn_permlane32_swap(__float_as_uint(v), __float_as_uint(v), false, false); return __uint_as_float(r[0]) + __uint_as_float(r[1]); }
DEVI float swap16_add(float v) { auto r = __builtin_amdgcn_permlane16_swap(__float_as_uint(v), __float_as_uint(v), false, false); return __uint_as_float(r[0]) + __uint_as_float(r[1]); }
DEVI float swap32_max(float v) { auto r = __builtin_amdgcn_permlane32_swap(__float_as_uint(v), __float_as_uint(v), false, false); return fmaxf(__uint_as_float(r[0]), __uint_as_float(r[1])); }
DEVI float swap16_max(float v) { auto r = __builtin_amdgcn_permlane16_swap(__float_as_uint(v), __float_as_uint(v), false, false); return fmaxf(__uint_as_float(r[0]), __uint_as_float(r[1])); }
DEVI float max16(float v) {
  v = fmaxf(v, dpp<0xB1>(v)); v = fmaxf(v, dpp<0x4E>(v)); v = fmaxf(v, dpp<0x141>(v)); return fmaxf(v, dpp<0x140>(v));
}
DEVI void put_flag(unsigned char* ws, unsigned off, u64 tag) { __hip_atomic_store((AS1 u64*)(ws + off), tag, RLX_AGENT); }
template <int SL> DEVI void wait_flags(unsigned char* ws, unsigned off, u64 tag) {
  for (unsigned spins = 0; spins < SPIN_CAP; ++spins) {
    if (__all(__hip_atomic_load((AS1 u64*)(ws + off), RLX_AGENT) == tag)) break;
    __builtin_amdgcn_s_sleep(SL);
  }
  __builtin_amdgcn_fence(__ATOMIC_ACQUIRE, "wavefront");
  asm volatile("" ::: "memory");
}
#define LD_SC1(so, vo) __builtin_amdgcn_raw_buffer_load_b128(rs, (vo), (so), 16)
#define ST_SC1(v, so, vo) __builtin_amdgcn_raw_buffer_store_b128((v), rs, (vo) + (so), 0, 16)

DEVI void pv_tile(const f16v& p, u4 v0, u4 v1, f16v& o) {
  u4 p0 = {pk(p[0], p[1]), pk(p[2], p[3]), pk(p[4], p[5]), pk(p[6], p[7])};
  o = mfma32(v0, p0, o);
  u4 p1 = {pk(p[8], p[9]), pk(p[10], p[11]), pk(p[12], p[13]), pk(p[14], p[15])};
  o = mfma32(v1, p1, o);
}
DEVI void tile_slow(const char* kp, const char* vp, bool vreal, unsigned vc, u4 q, float& m, f16v& o) {
  u4 v0 = lds16(vp), v1 = lds16(vp + 512);
#pragma unroll
  for (int e = 0; e < 4; ++e) { v0[e] = vreal ? v0[e] : vc; v1[e] = vreal ? v1[e] : vc; }
  f16v z = {0};
  f16v s = mfma32(lds16(kp), q, z);
  float t = s[0];
#pragma unroll
  for (int i = 1; i < 16; ++i) t = fmaxf(t, s[i]);
  float mn = fmaxf(m, swap32_max(t));
  float al = __builtin_amdgcn_exp2f(m - mn);
  m = mn;
  f16v p;
#pragma unroll
  for (int i = 0; i < 16; ++i) { p[i] = __builtin_amdgcn_exp2f(s[i] - mn); o[i] *= al; }
  pv_tile(p, v0, v1, o);
}

__global__ __launch_bounds__(512, 2) void fused_block_kernel(
    const unsigned char* __restrict__ x, const unsigned char* __restrict__ wq, const unsigned char* __restrict__ wk,
    const unsigned char* __restrict__ wv, const unsigned char* __restrict__ wo, const unsigned char* __restrict__ w1,
    const unsigned char* __restrict__ b1, const unsigned char* __restrict__ w2, unsigned char* __restrict__ out,
    unsigned char* ws) {
  __shared__ __attribute__((aligned(16))) char smem[LDS_BYTES];
  const int tid = threadIdx.x, lane = tid & 63;
  const int w = __builtin_amdgcn_readfirstlane(tid >> 6);
  const int bid = blockIdx.x;
  const int rb = (bid & 7) * 4 + ((bid >> 3) & 3), j = bid >> 5;
  const int R0 = rb * 64;
  const bool kvp = j == (rb & 7);
  const int c16 = lane & 15, g4 = lane >> 4, r32 = lane & 31, h2 = lane >> 5;
  const __amdgpu_buffer_rsrc_t rs = __builtin_amdgcn_make_buffer_rsrc((void*)ws, 0, (int)WS_END, 0x00020000);
  STAMP(0);
  const u64 e0 = __hip_atomic_load((AS1 u64*)(ws + WS_E), RLX_AGENT);

  {
    const unsigned char* xr = x + (size_t)(R0 + 8 * w + (lane >> 3)) * 1024 + (lane & 7) * 16;
    u4 xv[8];
#pragma unroll
    for (int i = 0; i < 8; ++i) xv[i] = *(const u4*)(xr + i * 128);
    if (kvp) {
#pragma unroll
      for (int i = 0; i < 4; ++i)
        dma16(((i >> 1) ? wv : wk) + (size_t)((w * 2 + (i & 1)) * 64 + lane) * 16, smem + L_WKV + (i >> 1) * 16384 + (w * 2 + (i & 1)) * 1024);
    }
#pragma unroll
    for (int i = 0; i < 8; ++i) {
      const int k = i * 64 + w * 8 + (lane >> 3), pc = lane & 7;
      const int f = ((lane >> 4) & 1) | ((w & 1) << 1);
      dma16(wq + (size_t)k * 1024 + j * 128 + ((((pc >> 1) ^ f) << 1) | (pc & 1)) * 16, smem + L_WQ + (i * 8 + w) * 1024);
    }
    float s1 = 0.f, s2 = 0.f;
#pragma unroll
    for (int i = 0; i < 8; ++i)
#pragma unroll
      for (int e = 0; e < 4; ++e) { s1 = dot2(xv[i][e], 0x3f803f80u, s1); s2 = dot2(xv[i][e], xv[i][e], s2); }
    s1 += dpp<0xB1>(s1); s2 += dpp<0xB1>(s2);
    s1 += dpp<0x4E>(s1); s2 += dpp<0x4E>(s2);
    s1 += dpp<0x141>(s1); s2 += dpp<0x141>(s2);
    const float mean = s1 * (1.f / 512.f);
    const float rstd = __builtin_amdgcn_rsqf(fmaxf(s2 * (1.f / 512.f) - mean * mean, 0.f) + 1e-5f);
    const float nm = -mean * rstd;
    const int rl = 8 * w + (lane >> 3), q7 = lane & 7;
    char* xw = smem + L_XN + (((rl >> 4) * 16 + (q7 >> 2)) * 64 + (q7 & 3) * 16 + ((rl & 15) ^ q7)) * 16;
#pragma unroll
    for (int i = 0; i < 8; ++i) {
      u4 y;
#pragma unroll
      for (int e = 0; e < 4; ++e) y[e] = pk(fmaf(lo16(xv[i][e]), rstd, nm), fmaf(hi16(xv[i][e]), rstd, nm));
      *(u4*)(xw + i * 2048) = y;
    }
  }
  const u64 tag = (((u64)__builtin_amdgcn_readfirstlane((unsigned)(e0 >> 32)) << 32) | (unsigned)__builtin_amdgcn_readfirstlane((unsigned)e0)) + 1;
  STAMP(1);
  asm volatile("s_waitcnt vmcnt(8)" ::: "memory");
  bar();
  STAMP(2);
  const int xo0 = (g4 * 16 + (c16 ^ g4)) * 16, xo1 = (g4 * 16 + (c16 ^ (4 + g4))) * 16;
  if (kvp) {
    const char* wp = smem + L_WKV + (w & 1) * 16384 + (8 * g4 + (c16 >> 2)) * 32 + (c16 & 3) * 8;
    const char* xp = smem + L_XN + (w >> 1) * 16384;
    f4 a0 = {0}, a1 = {0};
    u4 wf[16], xf[16];
#pragma unroll
    for (int s = 0; s < 16; ++s) { wf[s] = ldstr2(wp + s * 1024, 128); xf[s] = lds16(xp + s * 1024 + ((s & 1) ? xo1 : xo0)); }
    __builtin_amdgcn_sched_barrier(0);
    if (w & 1) {
#pragma unroll
      for (int s = 0; s < 16; s += 2) { a0 = mfma16(xf[s], wf[s], a0); a1 = mfma16(xf[s + 1], wf[s + 1], a1); }
    } else {
#pragma unroll
      for (int s = 0; s < 16; s += 2) { a0 = mfma16(wf[s], xf[s], a0); a1 = mfma16(wf[s + 1], xf[s + 1], a1); }
    }
    a0 += a1;
    u2 kv = {pk(a0[0], a0[1]), pk(a0[2], a0[3])};
    const int key0 = R0 + 16 * (w >> 1);
    if (w & 1) {
      __builtin_amdgcn_raw_buffer_store_b64(kv, rs, WS_V + (key0 >> 4) * 512 + (g4 & 1) * 256 + c16 * 16 + (g4 >> 1) * 8, 0, 16);
      asm volatile("s_waitcnt vmcnt(1)" ::: "memory");
    } else {
      __builtin_amdgcn_raw_buffer_store_b64(kv, rs, WS_K + (key0 >> 5) * 1024 + ((g4 >> 1) * 32 + (key0 & 16) + c16) * 16 + (g4 & 1) * 8, 0, 16);
      float n2 = dot2(kv[0], kv[0], dot2(kv[1], kv[1], 0.f));
      n2 = max16(swap32_add(swap16_add(n2)));
      if (lane == 0) __builtin_amdgcn_raw_buffer_store_b32(__float_as_uint(n2), rs, WS_KMAX + (rb * 4 + (w >> 1)) * 4, 0, 16);
      asm volatile("s_waitcnt vmcnt(2)" ::: "memory");
    }
  } else {
    drain();
  }
  bar();
  STAMP(3);
  const int rt = w & 1, ha = w >> 1;
  u4 qf;
  {
    const int f = ((c16 >> 3) & 1) | ((g4 & 1) << 1);
    const char* wp = smem + L_WQ + (8 * g4 + (c16 >> 2)) * 128 + ((ha ^ f) * 2) * 16 + (c16 & 3) * 8;
    const char* xp = smem + L_XN + rt * 32768;
    f4 a0 = {0}, a1 = {0};
    u4 wf[16], x0[16], x1[16];
#define Q_READ(s) do { wf[s] = ldstr2(wp + (s) * 4096, 512); x0[s] = lds16(xp + (s) * 1024 + (((s) & 1) ? xo1 : xo0)); \
                       x1[s] = lds16(xp + 16384 + (s) * 1024 + (((s) & 1) ? xo1 : xo0)); } while (0)
    Q_READ(0); Q_READ(1); Q_READ(2);
#pragma unroll
    for (int s = 0; s < 16; ++s) {
      __builtin_amdgcn_sched_barrier(0);
      if (s + 3 < 16) Q_READ(s + 3);
      __builtin_amdgcn_sched_barrier(0);
      a0 = mfma16(wf[s], x0[s], a0);
      a1 = mfma16(wf[s], x1[s], a1);
      if (s == KVS && kvp) {
        drain();
        bar();
        if (tid == 0) put_flag(ws, WS_F1 + rb * 128, tag);
      }
    }
    const float qs = 0.25f * LOG2E;
    auto r0 = __builtin_amdgcn_permlane16_swap(pk(a0[0] * qs, a0[1] * qs), pk(a1[0] * qs, a1[1] * qs), false, false);
    auto r1 = __builtin_amdgcn_permlane16_swap(pk(a0[2] * qs, a0[3] * qs), pk(a1[2] * qs, a1[3] * qs), false, false);
    qf = u4{r0[0], r1[0], r0[1], r1[1]};
    *(u4*)(smem + L_QX + w * 1024 + lane * 16) = qf;
  }
  STAMP(4);
  if (w == 0) wait_flags<PSL1>(ws, WS_F1 + r32 * 128, tag);
  bar();
  STAMP(5);

  unsigned w1r[16][8];
  const __amdgpu_buffer_rsrc_t rw1 = __builtin_amdgcn_make_buffer_rsrc((void*)w1, 0, 512 * 3584, 0x00020000);
  const int w1o = (16 * (g4 >> 1) + 4 * (g4 & 1)) * 3584 + (224 * j + 32 * (w < 7 ? w : 6) + 2 * c16) * 2;
#define W1_LOAD1(s, e) w1r[s][e] = __builtin_amdgcn_raw_buffer_load_b32(rw1, w1o, (32 * (s) + 8 * ((e) >> 2) + ((e) & 3)) * 3584, 0)
#define W1_LOAD(s) _Pragma("unroll") for (int e = 0; e < 8; ++e) W1_LOAD1(s, e)
  f16v o;
  u2 xres0, xres1;
  {
    u4 st[8][2];
    const int so = (tid >> 8) * 32768 + (tid & 255) * 16;
#define KV_LOAD(i) do { st[i][0] = LD_SC1(WS_K + (i) * 4096, so); st[i][1] = LD_SC1(WS_V + (i) * 4096, so); } while (0)
#define KV_PUT(i) do { *(u4*)(smem + L_K + so + (i) * 4096) = st[i][0]; *(u4*)(smem + L_V + so + (i) * 4096) = st[i][1]; } while (0)
    KV_LOAD(0);
    const u2 km = __builtin_bit_cast(u2, __builtin_amdgcn_raw_buffer_load_b64(rs, WS_KMAX + lane * 8, 0, 16));
    const u4 qb = lds16(smem + L_QX + (w ^ 1) * 1024 + lane * 16);
    const float qn = fmaxf(swap32_add(dot2(qf[0], qf[0], dot2(qf[1], qf[1], dot2(qf[2], qf[2], dot2(qf[3], qf[3], 0.f))))),
                           swap32_add(dot2(qb[0], qb[0], dot2(qb[1], qb[1], dot2(qb[2], qb[2], dot2(qb[3], qb[3], 0.f))))));
    KV_PUT(0);
    float kn = fmaxf(__uint_as_float(km[0]), __uint_as_float(km[1]));
    kn = swap32_max(swap16_max(max16(kn)));
    const bool safe = __all(qn * kn <= SAFE_BOUND * SAFE_BOUND);
    bar();
    STAMP(6);
    const bool vreal = r32 < 16;
    const unsigned vc = (r32 == 16 || r32 == 20) ? 0x3f803f80u : 0u;
    const unsigned ka = (unsigned)(size_t)(AS3 char*)(smem + L_K + rt * 32768 + lane * 16);
    const unsigned va = (unsigned)(size_t)(AS3 char*)(smem + L_V + rt * 32768 + h2 * 256 + c16 * 16);
    const u64 vm = 0x0000ffff0000ffffull;
    u4 kf, v0 = {vc, vc, vc, vc}, v1 = v0, p0, p1 = {0, 0, 0, 0};
#define K_READ(u) asm volatile("ds_read_b128 %0, %1 offset:%2" : "=v"(kf) : "v"(ka), "i"((u) * 1024))
#define V_READ(v, u, hf) asm volatile("s_mov_b64 exec, %3\n\tds_read_b128 %0, %1 offset:%2\n\ts_mov_b64 exec, -1" : "+v"(v) : "v"(va), "i"((u) * 1024 + (hf) * 512), "s"(vm))
#define LGKM(n, v) asm volatile("s_waitcnt lgkmcnt(" #n ")")
#define SB() __builtin_amdgcn_sched_barrier(0)
#define EXPS(s, a, b) _Pragma("unroll") for (int i = a; i < b; ++i) asm volatile("v_exp_f32 %0, %0" : "+v"(s[i]))
#define PKS(s, b) u4{pk(s[b], s[b + 1]), pk(s[b + 2], s[b + 3]), pk(s[b + 4], s[b + 5]), pk(s[b + 6], s[b + 7])}
    const f16v zero = {0};
    f16v oa = zero, ob = zero, sa, sb;
    unsigned pf0 = 0, pf1 = 0;
#define PV(o, v, p) asm volatile("v_mfma_f32_32x32x16_bf16 %0, %1, %2, %0" : "+v"(o) : "v"(v), "v"(p))
#define QK(s, q) asm volatile("v_mfma_f32_32x32x16_bf16 %0, %1, %2, 0" : "=&v"(s) : "v"(kf), "v"(q))
    K_READ(0); V_READ(v0, 0, 0);
    LGKM(1, kf);
    QK(sa, qf);
    SB();
    asm volatile("s_nop 7\n\ts_nop 7\n\ts_nop 7" : "+v"(sa), "+v"(oa), "+v"(ob), "+v"(p1), "+v"(v1));
#pragma unroll
    for (int u = 0; u < 32; ++u) {
      SB();
      if (u == 3 || u == 8) bar();
      if (u >= 2 && u < 6) { KV_PUT(2 * u - 3); if (u < 5) KV_PUT(2 * u - 2); }
      if (u < 4) { KV_LOAD(2 * u + 1); if (u < 3) KV_LOAD(2 * u + 2); }
      W1_LOAD1(u >> 1, 4 * (u & 1)); W1_LOAD1(u >> 1, 4 * (u & 1) + 1);
      SB();
      EXPS(sa, 0, 2); SB();
      PV(ob, v1, p1); SB();
      V_READ(v1, u, 1);
      EXPS(sa, 2, 6); SB();
      QK(sb, qb); SB();
      if (u < 31) K_READ(u + 1); else K_READ(u);
      EXPS(sa, 6, 8); SB();
      p0 = PKS(sa, 0); SB();
      EXPS(sa, 8, 10);
      LGKM(2, v0); SB();
      PV(oa, v0, p0); SB();
      EXPS(sa, 10, 16); SB();
      p1 = PKS(sa, 8); SB();
      W1_LOAD1(u >> 1, 4 * (u & 1) + 2); W1_LOAD1(u >> 1, 4 * (u & 1) + 3);
      if (u == 9) {
        const unsigned char* xr = x + (size_t)(R0 + 32 * rt + r32) * 1024 + (64 * j + 16 * ha + 4 * h2) * 2;
        xres0 = *(const u2*)xr; xres1 = *(const u2*)(xr + 16);
        const int cl = (bid >> 3) & 3;
        pf0 = *(const unsigned*)((w < 7 ? w2 + (size_t)(cl * 448 + w * 64 + lane) * 1024
                                        : wo + (size_t)(cl * 128 + lane) * 1024) + j * 128);
        pf1 = *(const unsigned*)(wo + (size_t)(cl * 128 + 64 + lane) * 1024 + j * 128);
      }
      if (u == 16) asm volatile("" :: "v"(pf0), "v"(pf1));
      SB();
      EXPS(sb, 0, 2);
      LGKM(1, v1); SB();
      PV(oa, v1, p1); SB();
      EXPS(sb, 2, 6);
      LGKM(0, kf); SB();
      QK(sa, qf); SB();
      EXPS(sb, 6, 8); SB();
      p0 = PKS(sb, 0); SB();
      EXPS(sb, 8, 10); SB();
      PV(ob, v0, p0); SB();
      if (u < 31) V_READ(v0, u + 1, 0);
      EXPS(sb, 10, 16); SB();
      p1 = PKS(sb, 8);
    }
    asm volatile("s_nop 1");
    PV(ob, v1, p1);
    asm volatile("s_nop 7\n\ts_nop 7\n\ts_nop 7" : "+v"(oa), "+v"(ob));
    SB();
    char* xo = smem + L_OX + (w ^ 1) * 2304 + lane * 16;
    *(f4*)xo = f4{ob[0], ob[1], ob[2], ob[3]};
    *(f4*)(xo + 1024) = f4{ob[4], ob[5], ob[6], ob[7]};
    *(float*)(smem + L_OX + (w ^ 1) * 2304 + 2048 + lane * 4) = ob[8];
    if (!safe) {
      float m = -3.0e38f;
      oa = zero;
      const char* kp = smem + L_K + lane * 16;
      const char* vp = smem + L_V + h2 * 256 + c16 * 16;
#pragma unroll 1
      for (int t = 0; t < 64; ++t) tile_slow(kp + t * 1024, vp + t * 1024, vreal, vc, qf, m, oa);
    }
    bar();
    o = oa;
    if (safe) {
      const char* xi = smem + L_OX + w * 2304 + lane * 16;
      const f4 a = *(const f4*)xi, b = *(const f4*)(xi + 1024);
#pragma unroll
      for (int e = 0; e < 4; ++e) { o[e] += a[e]; o[4 + e] += b[e]; }
      o[8] += *(const float*)(smem + L_OX + w * 2304 + 2048 + lane * 4);
    }
  }
  STAMP(7);
  {
  int tid = threadIdx.x;
  asm volatile("" : "+v"(tid));
  const int lane = tid & 63, c16 = lane & 15, g4 = lane >> 4, h2 = lane >> 5;
  {
    const float il = __builtin_amdgcn_rcpf(o[8]);
    u4 of = {pk(o[0] * il, o[1] * il), pk(o[2] * il, o[3] * il), pk(o[4] * il, o[5] * il), pk(o[6] * il, o[7] * il)};
    ST_SC1(of, WS_O + ((rb * 2 + rt) * 32 + 4 * j + ha) * 1024, lane * 16);
  }
  drain();
  bar();
  if (tid == 0) put_flag(ws, WS_F2 + (rb * 8 + j) * 128, tag);
  STAMP(8);

#pragma unroll
  for (int i = 0; i < 8; ++i) {
    const int k = i * 64 + w * 8 + (lane >> 3), pc = lane & 7;
    dma16(wo + (size_t)k * 1024 + j * 128 + (pc ^ (((lane >> 4) & 1) << 2)) * 16, smem + L_WO + (i * 8 + w) * 1024);
  }
  u4 a1f[16][2];
  f4 cs0 = {0}, cs1 = {0};
#define W1_PERM(s) _Pragma("unroll") for (int e = 0; e < 4; ++e) { \
    a1f[s][0][e] = __builtin_amdgcn_perm(w1r[s][2 * e + 1], w1r[s][2 * e], 0x05040100u); \
    a1f[s][1][e] = __builtin_amdgcn_perm(w1r[s][2 * e + 1], w1r[s][2 * e], 0x07060302u); } \
    cs0 = mfma16(a1f[s][0], ones, cs0); cs1 = mfma16(a1f[s][1], ones, cs1)
  const u4 ones = {0x3f803f80u, 0x3f803f80u, 0x3f803f80u, 0x3f803f80u};
#pragma unroll
  for (int s = 0; s < 16; ++s) { W1_PERM(s); }
  const int kq = ha;
  wait_flags<PSL>(ws, WS_F2 + (rb * 8 + 2 * kq + (lane & 1)) * 128, tag);
  STAMP(9);
  float hv[8];
  {
    u4 ob[8];
#pragma unroll
    for (int i = 0; i < 8; ++i) ob[i] = LD_SC1(WS_O + ((rb * 2 + rt) * 32 + 8 * kq + i) * 1024, lane * 16);
    asm volatile("s_waitcnt vmcnt(8)" ::: "memory");
    bar();
    const int q = c16 >> 2, u = g4 & 1;
    const char* wp = smem + L_WO + (kq * 128 + 4 * h2 + q) * 128 + (c16 & 3) * 8;
    const int po0 = (u ^ ((q >> 1) << 1)) * 32, po1 = ((2 + u) ^ ((q >> 1) << 1)) * 32;
    f16v acc[2] = {{0}, {0}};
#pragma unroll
    for (int i = 0; i < 8; ++i) {
      acc[0] = mfma32(ldstr2(wp + i * 2048 + po0, 1024), ob[i], acc[0]);
      acc[1] = mfma32(ldstr2(wp + i * 2048 + po1, 1024), ob[i], acc[1]);
    }
    STAMP(10);
#pragma unroll
    for (int d = 0; d < 4; ++d) {
      if (d != kq) {
        char* rp = smem + L_RED1 + ((rt + 2 * d) * 4 + kq) * 2048 + lane * 16;
        const f16v& a = acc[d >> 1];
        const int b = 8 * (d & 1);
        *(f4*)rp = f4{a[b], a[b + 1], a[b + 2], a[b + 3]};
        *(f4*)(rp + 1024) = f4{a[b + 4], a[b + 5], a[b + 6], a[b + 7]};
      } else {
#pragma unroll
        for (int e = 0; e < 8; ++e) hv[e] = acc[d >> 1][8 * (d & 1) + e];
      }
    }
    bar();
#pragma unroll
    for (int d = 1; d < 4; ++d) {
      const char* rp = smem + L_RED1 + (w * 4 + ((kq + d) & 3)) * 2048 + lane * 16;
      f4 a = *(const f4*)rp, b = *(const f4*)(rp + 1024);
#pragma unroll
      for (int e = 0; e < 4; ++e) { hv[e] += a[e]; hv[4 + e] += b[e]; }
    }
    u4 hf = {pk(hv[0] + lo16(xres0[0]), hv[1] + hi16(xres0[0])), pk(hv[2] + lo16(xres0[1]), hv[3] + hi16(xres0[1])),
             pk(hv[4] + lo16(xres1[0]), hv[5] + hi16(xres1[0])), pk(hv[6] + lo16(xres1[1]), hv[7] + hi16(xres1[1]))};
    ST_SC1(hf, WS_H + ((rb * 2 + rt) * 32 + 4 * j + kq) * 1024, lane * 16);
  }
  drain();
  bar();
  if (tid == 0) put_flag(ws, WS_F3 + (rb * 8 + j) * 128, tag);
  STAMP(11);

  {
    const u4 b1v = *(const u4*)(b1 + (224 * j + 32 * (w < 7 ? w : 6) + 8 * g4) * 2);
    if (c16 == 0) {
      char* cp = smem + L_CS + (w * 4 + g4) * 64;
      *(f4*)cp = cs0; *(f4*)(cp + 16) = cs1;
      *(f4*)(cp + 32) = f4{lo16(b1v[0]), lo16(b1v[1]), lo16(b1v[2]), lo16(b1v[3])};
      *(f4*)(cp + 48) = f4{hi16(b1v[0]), hi16(b1v[1]), hi16(b1v[2]), hi16(b1v[3])};
    }
  }
  const unsigned char* w2p = w2 + (size_t)(224 * w + (lane >> 3)) * 1024 + j * 128 + ((lane & 7) ^ (((lane >> 4) & 1) << 2)) * 16;
#define W2_DMA(s, dst) _Pragma("unroll") for (int q = 0; q < 4; ++q) dma16(w2p + (size_t)(32 * (s) + 8 * q) * 1024, smem + (dst) + q * 1024)
  W2_DMA(0, L_W2A + w * 8192); W2_DMA(1, L_W2A + w * 8192 + 4096);
  wait_flags<PSL>(ws, WS_F3 + (rb * 8 + w) * 128, tag);
  STAMP(12);
  {
    u4 hb[8];
#pragma unroll
    for (int i = 0; i < 8; ++i) hb[i] = LD_SC1(WS_H + ((rb * 2 + (i >> 2)) * 32 + 4 * w + (i & 3)) * 1024, lane * 16);
#pragma unroll
    for (int i = 0; i < 8; ++i) *(u4*)(smem + L_H + ((i >> 2) * 32 + 4 * w + (i & 3)) * 1024 + lane * 16) = hb[i];
  }
  bar();
  STAMP(13);
  u2 hr0, hr1;
  {
    const int mt = w >> 1;
    const char* hp = smem + L_H + ((mt >> 1) * 32 + 4 * j + 2 * (w & 1) + (g4 >> 1)) * 1024 + (16 * (mt & 1) + c16) * 16 + (g4 & 1) * 8;
    hr0 = *(const u2*)hp; hr1 = *(const u2*)(hp + 512);
  }
  {
    const char* hp = smem + L_H + (g4 >> 1) * 1024 + ((g4 & 1) * 32 + c16) * 16;
#define H_READ(s, mt) lds16(hp + ((mt) >> 1) * 32768 + (s) * 2048 + ((mt) & 1) * 256)
    if (w == 7) {
      float s1[4] = {}, s2[4] = {};
#pragma unroll 2
      for (int s = 0; s < 16; ++s)
#pragma unroll
        for (int mt = 0; mt < 4; ++mt) {
          const u4 v = H_READ(s, mt);
#pragma unroll
          for (int e = 0; e < 4; ++e) { s1[mt] = dot2(v[e], 0x3f803f80u, s1[mt]); s2[mt] = dot2(v[e], v[e], s2[mt]); }
        }
#pragma unroll
      for (int mt = 0; mt < 4; ++mt) {
        const float mean = swap32_add(swap16_add(s1[mt])) * (1.f / 512.f);
        const float rstd = __builtin_amdgcn_rsqf(fmaxf(swap32_add(swap16_add(s2[mt])) * (1.f / 512.f) - mean * mean, 0.f) + 1e-5f);
        if (g4 == 0) *(f2*)(smem + L_ST + (16 * mt + c16) * 8) = f2{mean, rstd};
      }
      bar();
    } else {
      f4 acc[2][4] = {};
      u4 hb[2][4];
#pragma unroll
      for (int mt = 0; mt < 4; ++mt) hb[0][mt] = H_READ(0, mt);
#pragma unroll
      for (int s = 0; s < 16; ++s) {
        if (s < 15) {
#pragma unroll
          for (int mt = 0; mt < 4; ++mt) hb[(s + 1) & 1][mt] = H_READ(s + 1, mt);
        }
        SB();
#pragma unroll
        for (int mt = 0; mt < 4; ++mt) {
          acc[0][mt] = mfma16(a1f[s][0], hb[s & 1][mt], acc[0][mt]);
          acc[1][mt] = mfma16(a1f[s][1], hb[s & 1][mt], acc[1][mt]);
        }
        SB();
      }
      STAMP(14);
      bar();
      const char* cp = smem + L_CS + (w * 4 + g4) * 64;
      const f4 c0 = *(const f4*)cp, c1 = *(const f4*)(cp + 16), bb0 = *(const f4*)(cp + 32), bb1 = *(const f4*)(cp + 48);
#pragma unroll
      for (int mt = 0; mt < 4; ++mt) {
        const f2 st = *(const f2*)(smem + L_ST + (16 * mt + c16) * 8);
        float y[8];
#pragma unroll
        for (int e = 0; e < 8; ++e) {
          const float cs = (e & 1) ? c1[e >> 1] : c0[e >> 1];
          const float bb = (e & 1) ? bb1[e >> 1] : bb0[e >> 1];
          const float v = fmaf(st[1], fmaf(-st[0], cs, acc[e & 1][mt][e >> 1]), bb);
          const float ex = __builtin_amdgcn_exp2f(v * LOG2E);
          const float r = __builtin_amdgcn_rcpf(fmaf(ex, ex + 2.f, 2.f));
          y[e] = fmaf(-2.f, v * r, v);
        }
        ST_SC1((u4{pk(y[0], y[1]), pk(y[2], y[3]), pk(y[4], y[5]), pk(y[6], y[7])}), WS_ACT + ((rb * 4 + mt) * 56 + 7 * j + w) * 1024, lane * 16);
      }
    }
  }
  drain();
  bar();
  if (tid == 0) put_flag(ws, WS_F4 + (rb * 8 + j) * 128, tag);
  STAMP(15);

  {
    const int q = c16 >> 2, fl = ((q >> 1) & 1) << 2;
    const int ro = (8 * g4 + q) * 128, co0 = ((c16 & 3) ^ fl) * 16, co1 = ((4 + (c16 & 3)) ^ fl) * 16;
#define W2_FRAG(base, t) ldstr2(smem + (base) + ro + (((t) >> 1) ? co1 : co0) + ((t) & 1) * 8, 512)
    W2_DMA(2, L_H + w * 8192); W2_DMA(3, L_H + w * 8192 + 4096); W2_DMA(4, 131072 + w * 4096);
    u4 af[7][4];
#pragma unroll
    for (int t = 0; t < 4; ++t) { af[0][t] = W2_FRAG(L_W2A + w * 8192, t); af[1][t] = W2_FRAG(L_W2A + w * 8192 + 4096, t); }
    asm volatile("s_waitcnt lgkmcnt(0)" ::: "memory");
    W2_DMA(5, L_W2A + w * 8192); W2_DMA(6, L_W2A + w * 8192 + 4096);
    wait_flags<PSL>(ws, WS_F4 + (rb * 8 + w) * 128, tag);
    STAMP(16);
    f4 acc[4][4] = {};
    u4 ab[7][4];
#define ACT_LOAD(s) _Pragma("unroll") for (int mt = 0; mt < 4; ++mt) ab[s][mt] = LD_SC1(WS_ACT + ((rb * 4 + mt) * 56 + 7 * w + (s)) * 1024, lane * 16)
    drain();
    ACT_LOAD(0); ACT_LOAD(1); ACT_LOAD(2); ACT_LOAD(3);
#pragma unroll
    for (int s = 0; s < 7; ++s) {
      SB();
      if (s >= 1 && s < 6) {
        const int base = (s == 1 ? L_H : s == 2 ? L_H + 4096 : s == 4 ? L_W2A : L_W2A + 4096) + w * 8192;
#pragma unroll
        for (int t = 0; t < 4; ++t) af[s + 1][t] = W2_FRAG(s == 3 ? 131072 + w * 4096 : base, t);
      }
      SB();
#pragma unroll
      for (int mt = 0; mt < 4; ++mt)
#pragma unroll
        for (int t = 0; t < 4; ++t) acc[t][mt] = mfma16(af[s][t], ab[s][mt], acc[t][mt]);
      SB();
      if (s + 4 < 7) { ACT_LOAD(s + 4); }
    }
    bar();
    STAMP(17);
    float y[8];
#pragma unroll
    for (int d = 0; d < 8; ++d) {
      const int mt = d >> 1, b = 2 * (d & 1);
      if (d != w) {
        char* rp = smem + L_RED2 + (d * 8 + w) * 2048 + lane * 16;
        *(f4*)rp = acc[b][mt];
        *(f4*)(rp + 1024) = acc[b + 1][mt];
      } else {
#pragma unroll
        for (int e = 0; e < 8; ++e) y[e] = acc[b + (e >> 2)][mt][e & 3];
      }
    }
    bar();
#pragma unroll
    for (int d = 1; d < 8; ++d) {
      const char* rp = smem + L_RED2 + (w * 8 + ((w + d) & 7)) * 2048 + lane * 16;
      f4 a = *(const f4*)rp, b = *(const f4*)(rp + 1024);
#pragma unroll
      for (int e = 0; e < 4; ++e) { y[e] += a[e]; y[4 + e] += b[e]; }
    }
    u4 r = {pk(y[0] + lo16(hr0[0]), y[1] + hi16(hr0[0])), pk(y[2] + lo16(hr0[1]), y[3] + hi16(hr0[1])),
            pk(y[4] + lo16(hr1[0]), y[5] + hi16(hr1[0])), pk(y[6] + lo16(hr1[1]), y[7] + hi16(hr1[1]))};
    *(u4*)(out + (size_t)(R0 + 16 * (w >> 1) + c16) * 1024 + (64 * j + 32 * (w & 1) + 8 * g4) * 2) = r;
  }
  if (bid == 0 && tid == 0) put_flag(ws, WS_E, tag);
  STAMP(18);
  }
}

extern "C" void kernel_launch(void* const* d_in, const int* in_sizes, int n_in, void* d_out, int out_size, void* d_ws,
                              size_t ws_size, hipStream_t stream) {
  typedef const unsigned char* cp;
  fused_block_kernel<<<256, 512, 0, stream>>>((cp)d_in[0], (cp)d_in[1], (cp)d_in[2], (cp)d_in[3], (cp)d_in[4], (cp)d_in[6],
                                              (cp)d_in[7], (cp)d_in[8], (unsigned char*)d_out, (unsigned char*)d_ws);
}
```

```cpp
#include <hip/hip_runtime.h>
#include <hip/hip_bf16.h>

typedef unsigned long long u64;
typedef __attribute__((ext_vector_type(8))) __bf16 bf8;
typedef __attribute__((ext_vector_type(2))) __bf16 bf2;
typedef __attribute__((ext_vector_type(4))) short s4;
typedef __attribute__((ext_vector_type(2))) float f2;
typedef __attribute__((ext_vector_type(4))) float f4;
typedef __attribute__((ext_vector_type(16))) float f16v;
typedef __attribute__((ext_vector_type(2))) unsigned u2;
typedef __attribute__((ext_vector_type(4))) unsigned u4;
#define AS1 __attribute__((address_space(1)))
#define AS3 __attribute__((address_space(3)))
#define DEVI __device__ __forceinline__
#define RLX_AGENT __ATOMIC_RELAXED, __HIP_MEMORY_SCOPE_AGENT

constexpr unsigned WS_E = 0, WS_F1 = 4096, WS_KMAX = 20480;
constexpr unsigned WS_F2 = 0x30000, WS_F3 = 0x38000, WS_F4 = 0x40000;
constexpr unsigned WS_K = 65536, WS_V = 131072, WS_O = 1u << 20, WS_H = 3u << 20, WS_ACT = 5u << 20, WS_END = 16u << 20;
constexpr int L_XN = 0, L_WQ = 65536, L_WKV = 131072, L_QX = 131072;
constexpr int L_K = 0, L_V = 65536, L_T1 = 131072;
constexpr int L_WO = 0, L_RED1 = 65536;
constexpr int L_H = 0, L_W2A = 65536, L_ST = 159744, L_CS = 160768;
constexpr int L_RED2 = 0;
constexpr int LDS_BYTES = 163840;
constexpr unsigned SPIN_CAP = 400000;
constexpr float LOG2E = 1.4426950408889634f;
constexpr float SAFE_BOUND = 50.0f;

#define PFW 0
#define KVS 2
#define PSL 2
#define PSL1 4
#define STAMP(i) do {} while (0)

DEVI void bar() { asm volatile("s_waitcnt lgkmcnt(0)\n\ts_barrier" ::: "memory"); }
DEVI void drain() { asm volatile("s_waitcnt vmcnt(0)" ::: "memory"); }
DEVI unsigned pk(float a, float b) { f2 v = {a, b}; return __builtin_bit_cast(unsigned, __builtin_convertvector(v, bf2)); }
DEVI float lo16(unsigned v) { return __uint_as_float(v << 16); }
DEVI float hi16(unsigned v) { return __uint_as_float(v & 0xffff0000u); }
DEVI float dot2(unsigned a, unsigned b, float c) {
  return __builtin_amdgcn_fdot2_f32_bf16(__builtin_bit_cast(bf2, a), __builtin_bit_cast(bf2, b), c, false);
}
DEVI f16v mfma32(u4 a, u4 b, f16v c) {
  return __builtin_amdgcn_mfma_f32_32x32x16_bf16(__builtin_bit_cast(bf8, a), __builtin_bit_cast(bf8, b), c, 0, 0, 0);
}
DEVI f4 mfma16(u4 a, u4 b, f4 c) {
  return __builtin_amdgcn_mfma_f32_16x16x32_bf16(__builtin_bit_cast(bf8, a), __builtin_bit_cast(bf8, b), c, 0, 0, 0);
}
DEVI u4 lds16(const char* p) { return *(const u4*)p; }
DEVI u2 ldstr(const char* p) { return __builtin_bit_cast(u2, __builtin_amdgcn_ds_read_tr16_b64_v4i16((AS3 s4*)p)); }
DEVI u4 ldstr2(const char* p, int d) { u2 a = ldstr(p), b = ldstr(p + d); return u4{a[0], a[1], b[0], b[1]}; }
DEVI void dma16(const void* g, char* l) { __builtin_amdgcn_global_load_lds((const AS1 void*)g, (AS3 void*)l, 16, 0, 0); }
template <int C> DEVI float dpp(float v) { return __int_as_float(__builtin_amdgcn_update_dpp(0, __float_as_int(v), C, 0xf, 0xf, true)); }
DEVI float swap32_add(float v) { auto r = __builtin_amdgcn_permlane32_swap(__float_as_uint(v), __float_as_uint(v), false, false); return __uint_as_float(r[0]) + __uint_as_float(r[1]); }
DEVI float swap16_add(float v) { auto r = __builtin_amdgcn_permlane16_swap(__float_as_uint(v), __float_as_uint(v), false, false); return __uint_as_float(r[0]) + __uint_as_float(r[1]); }
DEVI float swap32_max(float v) { auto r = __builtin_amdgcn_permlane32_swap(__float_as_uint(v), __float_as_uint(v), false, false); return fmaxf(__uint_as_float(r[0]), __uint_as_float(r[1])); }
DEVI float swap16_max(float v) { auto r = __builtin_amdgcn_permlane16_swap(__float_as_uint(v), __float_as_uint(v), false, false); return fmaxf(__uint_as_float(r[0]), __uint_as_float(r[1])); }
DEVI float max16(float v) {
  v = fmaxf(v, dpp<0xB1>(v)); v = fmaxf(v, dpp<0x4E>(v)); v = fmaxf(v, dpp<0x141>(v)); return fmaxf(v, dpp<0x140>(v));
}
DEVI void put_flag(unsigned char* ws, unsigned off, u64 tag) { __hip_atomic_store((AS1 u64*)(ws + off), tag, RLX_AGENT); }
template <int SL> DEVI void wait_flags(unsigned char* ws, unsigned off, u64 tag) {
  for (unsigned spins = 0; spins < SPIN_CAP; ++spins) {
    if (__all(__hip_atomic_load((AS1 u64*)(ws + off), RLX_AGENT) == tag)) break;
    __builtin_amdgcn_s_sleep(SL);
  }
  __builtin_amdgcn_fence(__ATOMIC_ACQUIRE, "wavefront");
  asm volatile("" ::: "memory");
}
#define LD_SC1(so, vo) __builtin_amdgcn_raw_buffer_load_b128(rs, (vo), (so), 16)
#define ST_SC1(v, so, vo) __builtin_amdgcn_raw_buffer_store_b128((v), rs, (vo) + (so), 0, 16)

DEVI void pv_tile(const f16v& p, u4 v0, u4 v1, f16v& o) {
  u4 p0 = {pk(p[0], p[1]), pk(p[2], p[3]), pk(p[4], p[5]), pk(p[6], p[7])};
  o = mfma32(v0, p0, o);
  u4 p1 = {pk(p[8], p[9]), pk(p[10], p[11]), pk(p[12], p[13]), pk(p[14], p[15])};
  o = mfma32(v1, p1, o);
}
DEVI void tile_slow(const char* kp, const char* vp, bool vreal, unsigned vc, u4 q, float& m, f16v& o) {
  u4 v0 = lds16(vp), v1 = lds16(vp + 512);
#pragma unroll
  for (int e = 0; e < 4; ++e) { v0[e] = vreal ? v0[e] : vc; v1[e] = vreal ? v1[e] : vc; }
  f16v z = {0};
  f16v s = mfma32(lds16(kp), q, z);
  float t = s[0];
#pragma unroll
  for (int i = 1; i < 16; ++i) t = fmaxf(t, s[i]);
  float mn = fmaxf(m, swap32_max(t));
  float al = __builtin_amdgcn_exp2f(m - mn);
  m = mn;
  f16v p;
#pragma unroll
  for (int i = 0; i < 16; ++i) { p[i] = __builtin_amdgcn_exp2f(s[i] - mn); o[i] *= al; }
  pv_tile(p, v0, v1, o);
}

__global__ __launch_bounds__(512, 2) void fused_block_kernel(
    const unsigned char* __restrict__ x, const unsigned char* __restrict__ wq, const unsigned char* __restrict__ wk,
    const unsigned char* __restrict__ wv, const unsigned char* __restrict__ wo, const unsigned char* __restrict__ w1,
    const unsigned char* __restrict__ b1, const unsigned char* __restrict__ w2, unsigned char* __restrict__ out,
    unsigned char* ws) {
  __shared__ __attribute__((aligned(16))) char smem[LDS_BYTES];
  const int tid = threadIdx.x, lane = tid & 63;
  const int w = __builtin_amdgcn_readfirstlane(tid >> 6);
  const int bid = blockIdx.x;
  const int rb = (bid & 7) * 4 + ((bid >> 3) & 3), j = bid >> 5;
  const int R0 = rb * 64;
  const bool kvp = j == (rb & 7);
  const int c16 = lane & 15, g4 = lane >> 4, r32 = lane & 31, h2 = lane >> 5;
  const __amdgpu_buffer_rsrc_t rs = __builtin_amdgcn_make_buffer_rsrc((void*)ws, 0, (int)WS_END, 0x00020000);
  STAMP(0);
  const u64 e0 = __hip_atomic_load((AS1 u64*)(ws + WS_E), RLX_AGENT);

  {
    const unsigned char* xr = x + (size_t)(R0 + 8 * w + (lane >> 3)) * 1024 + (lane & 7) * 16;
    u4 xv[8];
#pragma unroll
    for (int i = 0; i < 8; ++i) xv[i] = *(const u4*)(xr + i * 128);
    if (kvp) {
#pragma unroll
      for (int i = 0; i < 4; ++i)
        dma16(((i >> 1) ? wv : wk) + (size_t)((w * 2 + (i & 1)) * 64 + lane) * 16, smem + L_WKV + (i >> 1) * 16384 + (w * 2 + (i & 1)) * 1024);
    }
#pragma unroll
    for (int i = 0; i < 8; ++i) {
      const int k = i * 64 + w * 8 + (lane >> 3), pc = lane & 7;
      const int f = ((lane >> 4) & 1) | ((w & 1) << 1);
      dma16(wq + (size_t)k * 1024 + j * 128 + ((((pc >> 1) ^ f) << 1) | (pc & 1)) * 16, smem + L_WQ + (i * 8 + w) * 1024);
    }
    float s1 = 0.f, s2 = 0.f;
#pragma unroll
    for (int i = 0; i < 8; ++i)
#pragma unroll
      for (int e = 0; e < 4; ++e) { s1 = dot2(xv[i][e], 0x3f803f80u, s1); s2 = dot2(xv[i][e], xv[i][e], s2); }
    s1 += dpp<0xB1>(s1); s2 += dpp<0xB1>(s2);
    s1 += dpp<0x4E>(s1); s2 += dpp<0x4E>(s2);
    s1 += dpp<0x141>(s1); s2 += dpp<0x141>(s2);
    const float mean = s1 * (1.f / 512.f);
    const float rstd = __builtin_amdgcn_rsqf(fmaxf(s2 * (1.f / 512.f) - mean * mean, 0.f) + 1e-5f);
    const float nm = -mean * rstd;
    const int rl = 8 * w + (lane >> 3), q7 = lane & 7;
    char* xw = smem + L_XN + (((rl >> 4) * 16 + (q7 >> 2)) * 64 + (q7 & 3) * 16 + ((rl & 15) ^ q7)) * 16;
#pragma unroll
    for (int i = 0; i < 8; ++i) {
      u4 y;
#pragma unroll
      for (int e = 0; e < 4; ++e) y[e] = pk(fmaf(lo16(xv[i][e]), rstd, nm), fmaf(hi16(xv[i][e]), rstd, nm));
      *(u4*)(xw + i * 2048) = y;
    }
  }
  const u64 tag = (((u64)__builtin_amdgcn_readfirstlane((unsigned)(e0 >> 32)) << 32) | (unsigned)__builtin_amdgcn_readfirstlane((unsigned)e0)) + 1;
  STAMP(1);
  asm volatile("s_waitcnt vmcnt(8)" ::: "memory");
  bar();
  STAMP(2);
  const int xo0 = (g4 * 16 + (c16 ^ g4)) * 16, xo1 = (g4 * 16 + (c16 ^ (4 + g4))) * 16;
  if (kvp) {
    const char* wp = smem + L_WKV + (w & 1) * 16384 + (8 * g4 + (c16 >> 2)) * 32 + (c16 & 3) * 8;
    const char* xp = smem + L_XN + (w >> 1) * 16384;
    f4 a0 = {0}, a1 = {0};
    u4 wf[16], xf[16];
#pragma unroll
    for (int s = 0; s < 16; ++s) { wf[s] = ldstr2(wp + s * 1024, 128); xf[s] = lds16(xp + s * 1024 + ((s & 1) ? xo1 : xo0)); }
    __builtin_amdgcn_sched_barrier(0);
    if (w & 1) {
#pragma unroll
      for (int s = 0; s < 16; s += 2) { a0 = mfma16(xf[s], wf[s], a0); a1 = mfma16(xf[s + 1], wf[s + 1], a1); }
    } else {
#pragma unroll
      for (int s = 0; s < 16; s += 2) { a0 = mfma16(wf[s], xf[s], a0); a1 = mfma16(wf[s + 1], xf[s + 1], a1); }
    }
    a0 += a1;
    u2 kv = {pk(a0[0], a0[1]), pk(a0[2], a0[3])};
    const int key0 = R0 + 16 * (w >> 1);
    if (w & 1) {
      __builtin_amdgcn_raw_buffer_store_b64(kv, rs, WS_V + (key0 >> 4) * 512 + (g4 & 1) * 256 + c16 * 16 + (g4 >> 1) * 8, 0, 16);
      asm volatile("s_waitcnt vmcnt(1)" ::: "memory");
    } else {
      __builtin_amdgcn_raw_buffer_store_b64(kv, rs, WS_K + (key0 >> 5) * 1024 + ((g4 >> 1) * 32 + (key0 & 16) + c16) * 16 + (g4 & 1) * 8, 0, 16);
      float n2 = dot2(kv[0], kv[0], dot2(kv[1], kv[1], 0.f));
      n2 = max16(swap32_add(swap16_add(n2)));
      if (lane == 0) __builtin_amdgcn_raw_buffer_store_b32(__float_as_uint(n2), rs, WS_KMAX + (rb * 4 + (w >> 1)) * 4, 0, 16);
      asm volatile("s_waitcnt vmcnt(2)" ::: "memory");
    }
  } else {
    drain();
  }
  bar();
  STAMP(3);
  const int rt = w & 1, ha = w >> 1;
  u4 qf;
  {
    const int f = ((c16 >> 3) & 1) | ((g4 & 1) << 1);
    const char* wp = smem + L_WQ + (8 * g4 + (c16 >> 2)) * 128 + ((ha ^ f) * 2) * 16 + (c16 & 3) * 8;
    const char* xp = smem + L_XN + rt * 32768;
    f4 a0 = {0}, a1 = {0};
    u4 wf[16], x0[16], x1[16];
#define Q_READ(s) do { wf[s] = ldstr2(wp + (s) * 4096, 512); x0[s] = lds16(xp + (s) * 1024 + (((s) & 1) ? xo1 : xo0)); \
                       x1[s] = lds16(xp + 16384 + (s) * 1024 + (((s) & 1) ? xo1 : xo0)); } while (0)
    Q_READ(0); Q_READ(1); Q_READ(2);
#pragma unroll
    for (int s = 0; s < 16; ++s) {
      __builtin_amdgcn_sched_barrier(0);
      if (s + 3 < 16) Q_READ(s + 3);
      __builtin_amdgcn_sched_barrier(0);
      a0 = mfma16(wf[s], x0[s], a0);
      a1 = mfma16(wf[s], x1[s], a1);
      if (s == KVS && kvp) {
        drain();
        bar();
        if (tid == 0) put_flag(ws, WS_F1 + rb * 128, tag);
      }
    }
    const float qs = 0.25f * LOG2E;
    auto r0 = __builtin_amdgcn_permlane16_swap(pk(a0[0] * qs, a0[1] * qs), pk(a1[0] * qs, a1[1] * qs), false, false);
    auto r1 = __builtin_amdgcn_permlane16_swap(pk(a0[2] * qs, a0[3] * qs), pk(a1[2] * qs, a1[3] * qs), false, false);
    qf = u4{r0[0], r1[0], r0[1], r1[1]};
    *(u4*)(smem + L_QX + w * 1024 + lane * 16) = qf;
  }
  STAMP(4);
  if (w == 0) wait_flags<PSL1>(ws, WS_F1 + r32 * 128, tag);
  if (PFW && w >= 4) {
    const unsigned char* pp = w1 + (size_t)(((bid >> 3) & 3) * 128 + ((tid & 255) >> 1)) * 3584 + 448 * j + (tid & 1) * 256;
    unsigned pa, pb;
    asm volatile("global_load_dword %0, %2, off\n\tglobal_load_dword %1, %2, off offset:128\n\ts_waitcnt vmcnt(0)" : "=&v"(pa), "=&v"(pb) : "v"(pp) : "memory");
  }
  bar();
  STAMP(5);

  u2 w1f[16][4];
  const __amdgpu_buffer_rsrc_t rw1 = __builtin_amdgcn_make_buffer_rsrc((void*)w1, 0, 512 * 3584, 0x00020000);
  const int w1o = w < 7 ? (lane >> 2) * 3584 + (224 * j + 32 * w) * 2 + ((lane & 3) ^ ((((lane >> 3) ^ (lane >> 4)) & 1) << 1)) * 16 : 0x40000000;
#define W1_DMA(i) __builtin_amdgcn_raw_ptr_buffer_load_lds(rw1, (AS3 void*)(smem + L_T1 + w * 4096 + ((i) & 3) * 1024), 16, w1o, 16 * (i) * 3584, 0, 0)
  const unsigned t0 = (unsigned)(size_t)(AS3 char*)(smem + L_T1 + w * 4096 + (16 * (g4 >> 1) + 4 * (g4 & 1) + (c16 >> 2)) * 64 +
                                                    ((((c16 >> 3) ^ g4) & 1) * 2 + ((c16 >> 1) & 1)) * 16 + (c16 & 1) * 8);
  const unsigned t1 = t0 ^ 32;
#define W1_READ(s, n) asm volatile("s_waitcnt vmcnt(%8)\n\tds_read_b64_tr_b16 %0, %4 offset:%6\n\tds_read_b64_tr_b16 %1, %4 offset:%7\n\t" \
    "ds_read_b64_tr_b16 %2, %5 offset:%6\n\tds_read_b64_tr_b16 %3, %5 offset:%7" \
    : "=&v"(w1f[s][0]), "=&v"(w1f[s][1]), "=&v"(w1f[s][2]), "=&v"(w1f[s][3]) : "v"(t0), "v"(t1), "i"(((s) & 1) * 2048), "i"(((s) & 1) * 2048 + 512), "i"(n))
  f16v o;
  u2 xres0, xres1;
  {
    u4 st[8][2];
    const int so = (tid >> 8) * 32768 + (tid & 255) * 16;
#define KV_LOAD(i) do { st[i][0] = LD_SC1(WS_K + (i) * 4096, so); st[i][1] = LD_SC1(WS_V + (i) * 4096, so); } while (0)
#define KV_PUT(i) do { *(u4*)(smem + L_K + so + (i) * 4096) = st[i][0]; *(u4*)(smem + L_V + so + (i) * 4096) = st[i][1]; } while (0)
    KV_LOAD(0);
    const u2 km = __builtin_bit_cast(u2, __builtin_amdgcn_raw_buffer_load_b64(rs, WS_KMAX + lane * 8, 0, 16));
    const u4 qb = lds16(smem + L_QX + (w ^ 1) * 1024 + lane * 16);
    const float qn = fmaxf(swap32_add(dot2(qf[0], qf[0], dot2(qf[1], qf[1], dot2(qf[2], qf[2], dot2(qf[3], qf[3], 0.f))))),
                           swap32_add(dot2(qb[0], qb[0], dot2(qb[1], qb[1], dot2(qb[2], qb[2], dot2(qb[3], qb[3], 0.f))))));
    KV_PUT(0);
    float kn = fmaxf(__uint_as_float(km[0]), __uint_as_float(km[1]));
    kn = swap32_max(swap16_max(max16(kn)));
    const bool safe = __all(qn * kn <= SAFE_BOUND * SAFE_BOUND);
    bar();
    STAMP(6);
    const bool vreal = r32 < 16;
    const unsigned vc = (r32 == 16 || r32 == 20) ? 0x3f803f80u : 0u;
    const unsigned ka = (unsigned)(size_t)(AS3 char*)(smem + L_K + rt * 32768 + lane * 16);
    const unsigned va = (unsigned)(size_t)(AS3 char*)(smem + L_V + rt * 32768 + h2 * 256 + c16 * 16);
    const u64 vm = 0x0000ffff0000ffffull;
    u4 kf, v0 = {vc, vc, vc, vc}, v1 = v0, p0, p1 = {0, 0, 0, 0};
#define K_READ(u) asm volatile("ds_read_b128 %0, %1 offset:%2" : "=v"(kf) : "v"(ka), "i"((u) * 1024))
#define V_READ(v, u, hf) asm volatile("s_mov_b64 exec, %3\n\tds_read_b128 %0, %1 offset:%2\n\ts_mov_b64 exec, -1" : "+v"(v) : "v"(va), "i"((u) * 1024 + (hf) * 512), "s"(vm))
#define LGKM(n, v) asm volatile("s_waitcnt lgkmcnt(" #n ")")
#define SB() __builtin_amdgcn_sched_barrier(0)
#define EXPS(s, a, b) _Pragma("unroll") for (int i = a; i < b; ++i) asm volatile("v_exp_f32 %0, %0" : "+v"(s[i]))
#define PKS(s, b) u4{pk(s[b], s[b + 1]), pk(s[b + 2], s[b + 3]), pk(s[b + 4], s[b + 5]), pk(s[b + 6], s[b + 7])}
    const f16v zero = {0};
    f16v oa = zero, ob = zero, sa, sb;
    unsigned pf0 = 0, pf1 = 0;
#define PV(o, v, p) asm volatile("v_mfma_f32_32x32x16_bf16 %0, %1, %2, %0" : "+v"(o) : "v"(v), "v"(p))
#define QK(s, q) asm volatile("v_mfma_f32_32x32x16_bf16 %0, %1, %2, 0" : "=&v"(s) : "v"(kf), "v"(q))
    W1_DMA(0); W1_DMA(1);
    K_READ(0); V_READ(v0, 0, 0);
    LGKM(1, kf);
    QK(sa, qf);
    SB();
    asm volatile("s_nop 7\n\ts_nop 7\n\ts_nop 7" : "+v"(sa), "+v"(oa), "+v"(ob), "+v"(p1), "+v"(v1));
#pragma unroll
    for (int u = 0; u < 32; ++u) {
      SB();
      if (u == 3 || u == 8) bar();
      if (u >= 2 && u < 6) { KV_PUT(2 * u - 3); if (u < 5) KV_PUT(2 * u - 2); }
      if (u < 4) { KV_LOAD(2 * u + 1); if (u < 3) KV_LOAD(2 * u + 2); }
      if (u < 30) W1_DMA(u + 2);
      SB();
      EXPS(sa, 0, 2); SB();
      PV(ob, v1, p1); SB();
      V_READ(v1, u, 1);
      EXPS(sa, 2, 6); SB();
      QK(sb, qb); SB();
      if (u < 31) K_READ(u + 1); else K_READ(u);
      if (u & 1) W1_READ(u >> 1, u == 1 ? 10 : u == 3 ? 8 : u == 11 ? 6 : u == 31 ? 0 : 2);
      EXPS(sa, 6, 8); SB();
      p0 = PKS(sa, 0); SB();
      EXPS(sa, 8, 10);
      if (u & 1) LGKM(6, v0); else LGKM(2, v0);
      SB();
      PV(oa, v0, p0); SB();
      EXPS(sa, 10, 16); SB();
      p1 = PKS(sa, 8); SB();
      if (u == 9) {
        const unsigned char* xr = x + (size_t)(R0 + 32 * rt + r32) * 1024 + (64 * j + 16 * ha + 4 * h2) * 2;
        xres0 = *(const u2*)xr; xres1 = *(const u2*)(xr + 16);
        const int cl = (bid >> 3) & 3;
        pf0 = *(const unsigned*)((w < 7 ? w2 + (size_t)(cl * 448 + w * 64 + lane) * 1024
                                        : wo + (size_t)(cl * 128 + lane) * 1024) + j * 128);
        pf1 = *(const unsigned*)(wo + (size_t)(cl * 128 + 64 + lane) * 1024 + j * 128);
      }
      if (u == 16) asm volatile("" :: "v"(pf0), "v"(pf1));

      SB();
      EXPS(sb, 0, 2);
      if (u & 1) LGKM(5, v1); else LGKM(1, v1);
      SB();
      PV(oa, v1, p1); SB();
      EXPS(sb, 2, 6);
      LGKM(0, kf);
      if (u & 1) asm volatile("" : "+v"(w1f[u >> 1][0]), "+v"(w1f[u >> 1][1]), "+v"(w1f[u >> 1][2]), "+v"(w1f[u >> 1][3]));
      SB();
      QK(sa, qf); SB();
      EXPS(sb, 6, 8); SB();
      p0 = PKS(sb, 0); SB();
      EXPS(sb, 8, 10); SB();
      PV(ob, v0, p0); SB();
      if (u < 31) V_READ(v0, u + 1, 0);
      EXPS(sb, 10, 16); SB();
      p1 = PKS(sb, 8);
    }
    asm volatile("s_nop 1");
    PV(ob, v1, p1);
    asm volatile("s_nop 7\n\ts_nop 7\n\ts_nop 7" : "+v"(oa), "+v"(ob));
    SB();
    char* xo = smem + L_T1 + w * 4096 + lane * 16;
    *(f4*)xo = f4{ob[0], ob[1], ob[2], ob[3]};
    *(f4*)(xo + 1024) = f4{ob[4], ob[5], ob[6], ob[7]};
    *(float*)(smem + L_T1 + w * 4096 + 2048 + lane * 4) = ob[8];
    if (!safe) {
      float m = -3.0e38f;
      oa = zero;
      const char* kp = smem + L_K + lane * 16;
      const char* vp = smem + L_V + h2 * 256 + c16 * 16;
#pragma unroll 1
      for (int t = 0; t < 64; ++t) tile_slow(kp + t * 1024, vp + t * 1024, vreal, vc, qf, m, oa);
    }
    bar();
    o = oa;
    if (safe) {
      const char* xi = smem + L_T1 + (w ^ 1) * 4096 + lane * 16;
      const f4 a = *(const f4*)xi, b = *(const f4*)(xi + 1024);
#pragma unroll
      for (int e = 0; e < 4; ++e) { o[e] += a[e]; o[4 + e] += b[e]; }
      o[8] += *(const float*)(smem + L_T1 + (w ^ 1) * 4096 + 2048 + lane * 4);
    }
  }
  STAMP(7);
  {
  int tid = threadIdx.x;
  asm volatile("" : "+v"(tid));
  const int lane = tid & 63, c16 = lane & 15, g4 = lane >> 4, h2 = lane >> 5;
  {
    const float il = __builtin_amdgcn_rcpf(o[8]);
    u4 of = {pk(o[0] * il, o[1] * il), pk(o[2] * il, o[3] * il), pk(o[4] * il, o[5] * il), pk(o[6] * il, o[7] * il)};
    ST_SC1(of, WS_O + ((rb * 2 + rt) * 32 + 4 * j + ha) * 1024, lane * 16);
  }
  asm volatile("" ::: "memory");
#pragma unroll
  for (int i = 0; i < 8; ++i) {
    const int k = i * 64 + w * 8 + (lane >> 3), pc = lane & 7;
    dma16(wo + (size_t)k * 1024 + j * 128 + (pc ^ (((lane >> 4) & 1) << 2)) * 16, smem + L_WO + (i * 8 + w) * 1024);
  }
  asm volatile("s_waitcnt vmcnt(8)" ::: "memory");
  bar();
  if (tid == 0) put_flag(ws, WS_F2 + (rb * 8 + j) * 128, tag);
  STAMP(8);

  u4 a1f[16][2];
  f4 cs0 = {0}, cs1 = {0};
  const u4 ones = {0x3f803f80u, 0x3f803f80u, 0x3f803f80u, 0x3f803f80u};
#pragma unroll
  for (int s = 0; s < 16; ++s) {
    a1f[s][0] = u4{w1f[s][0][0], w1f[s][0][1], w1f[s][1][0], w1f[s][1][1]};
    a1f[s][1] = u4{w1f[s][2][0], w1f[s][2][1], w1f[s][3][0], w1f[s][3][1]};
    cs0 = mfma16(a1f[s][0], ones, cs0); cs1 = mfma16(a1f[s][1], ones, cs1);
  }
  const unsigned char* bp = b1 + (224 * j + 32 * (w < 7 ? w : 6) + 4 * g4) * 2;
  const u2 ba = *(const u2*)bp, bb = *(const u2*)(bp + 32);
  if (c16 == 0) {
    char* cp = smem + L_CS + (w * 4 + g4) * 64;
    *(f4*)cp = cs0; *(f4*)(cp + 16) = cs1;
    *(f4*)(cp + 32) = f4{lo16(ba[0]), hi16(ba[0]), lo16(ba[1]), hi16(ba[1])};
    *(f4*)(cp + 48) = f4{lo16(bb[0]), hi16(bb[0]), lo16(bb[1]), hi16(bb[1])};
  }
  const int kq = ha;
  wait_flags<PSL>(ws, WS_F2 + (rb * 8 + 2 * kq + (lane & 1)) * 128, tag);
  STAMP(9);
  float hv[8];
  {
    u4 ob[8];
#pragma unroll
    for (int i = 0; i < 8; ++i) ob[i] = LD_SC1(WS_O + ((rb * 2 + rt) * 32 + 8 * kq + i) * 1024, lane * 16);
    asm volatile("s_waitcnt vmcnt(8)" ::: "memory");
    bar();
    const int q = c16 >> 2, u = g4 & 1;
    const char* wp = smem + L_WO + (kq * 128 + 4 * h2 + q) * 128 + (c16 & 3) * 8;
    const int po0 = (u ^ ((q >> 1) << 1)) * 32, po1 = ((2 + u) ^ ((q >> 1) << 1)) * 32;
    f16v acc[2] = {{0}, {0}};
#pragma unroll
    for (int i = 0; i < 8; ++i) {
      acc[0] = mfma32(ldstr2(wp + i * 2048 + po0, 1024), ob[i], acc[0]);
      acc[1] = mfma32(ldstr2(wp + i * 2048 + po1, 1024), ob[i], acc[1]);
    }
    STAMP(10);
#pragma unroll
    for (int d = 0; d < 4; ++d) {
      if (d != kq) {
        char* rp = smem + L_RED1 + ((rt + 2 * d) * 4 + kq) * 2048 + lane * 16;
        const f16v& a = acc[d >> 1];
        const int b = 8 * (d & 1);
        *(f4*)rp = f4{a[b], a[b + 1], a[b + 2], a[b + 3]};
        *(f4*)(rp + 1024) = f4{a[b + 4], a[b + 5], a[b + 6], a[b + 7]};
      } else {
#pragma unroll
        for (int e = 0; e < 8; ++e) hv[e] = acc[d >> 1][8 * (d & 1) + e];
      }
    }
    bar();
#pragma unroll
    for (int d = 1; d < 4; ++d) {
      const char* rp = smem + L_RED1 + (w * 4 + ((kq + d) & 3)) * 2048 + lane * 16;
      f4 a = *(const f4*)rp, b = *(const f4*)(rp + 1024);
#pragma unroll
      for (int e = 0; e < 4; ++e) { hv[e] += a[e]; hv[4 + e] += b[e]; }
    }
    u4 hf = {pk(hv[0] + lo16(xres0[0]), hv[1] + hi16(xres0[0])), pk(hv[2] + lo16(xres0[1]), hv[3] + hi16(xres0[1])),
             pk(hv[4] + lo16(xres1[0]), hv[5] + hi16(xres1[0])), pk(hv[6] + lo16(xres1[1]), hv[7] + hi16(xres1[1]))};
    ST_SC1(hf, WS_H + ((rb * 2 + rt) * 32 + 4 * j + kq) * 1024, lane * 16);
  }
  drain();
  bar();
  if (tid == 0) put_flag(ws, WS_F3 + (rb * 8 + j) * 128, tag);
  STAMP(11);

  const unsigned char* w2p = w2 + (size_t)(224 * w + (lane >> 3)) * 1024 + j * 128 + ((lane & 7) ^ (((lane >> 4) & 1) << 2)) * 16;
#define W2_DMA(s, dst) _Pragma("unroll") for (int q = 0; q < 4; ++q) dma16(w2p + (size_t)(32 * (s) + 8 * q) * 1024, smem + (dst) + q * 1024)
  W2_DMA(0, L_W2A + w * 8192); W2_DMA(1, L_W2A + w * 8192 + 4096);
  wait_flags<PSL>(ws, WS_F3 + (rb * 8 + w) * 128, tag);
  STAMP(12);
  {
    u4 hb[8];
#pragma unroll
    for (int i = 0; i < 8; ++i) hb[i] = LD_SC1(WS_H + ((rb * 2 + (i >> 2)) * 32 + 4 * w + (i & 3)) * 1024, lane * 16);
#pragma unroll
    for (int i = 0; i < 8; ++i) *(u4*)(smem + L_H + ((i >> 2) * 32 + 4 * w + (i & 3)) * 1024 + lane * 16) = hb[i];
  }
  bar();
  STAMP(13);
  u4 af[7][4];
  const int ro = (4 * g4 + (c16 >> 2)) * 128, co0 = ((c16 & 3) ^ (((c16 >> 3) & 1) << 2)) * 16, co1 = co0 ^ 64;
#define W2_FRAG(base, t) ldstr2(smem + (base) + ro + (((t) >> 1) ? co1 : co0) + ((t) & 1) * 8, 2048)
#define W2_NEXT() do { \
    _Pragma("unroll") for (int t = 0; t < 4; ++t) { af[0][t] = W2_FRAG(L_W2A + w * 8192, t); af[1][t] = W2_FRAG(L_W2A + w * 8192 + 4096, t); } \
    asm volatile("s_waitcnt lgkmcnt(0)" ::: "memory"); \
    W2_DMA(2, L_H + w * 8192); W2_DMA(3, L_H + w * 8192 + 4096); W2_DMA(5, L_W2A + w * 8192); W2_DMA(6, L_W2A + w * 8192 + 4096); } while (0)
  u2 hr0, hr1;
  {
    const int mt = w >> 1;
    const char* hp = smem + L_H + ((mt >> 1) * 32 + 4 * j + 2 * (w & 1) + (g4 >> 1)) * 1024 + (16 * (mt & 1) + c16) * 16 + (g4 & 1) * 8;
    hr0 = *(const u2*)hp; hr1 = *(const u2*)(hp + 512);
  }
  {
    const char* hp = smem + L_H + (g4 >> 1) * 1024 + ((g4 & 1) * 32 + c16) * 16;
#define H_READ(s, mt) lds16(hp + ((mt) >> 1) * 32768 + (s) * 2048 + ((mt) & 1) * 256)
    if (w == 7) {
      float s1[4] = {}, s2[4] = {};
#pragma unroll 2
      for (int s = 0; s < 16; ++s)
#pragma unroll
        for (int mt = 0; mt < 4; ++mt) {
          const u4 v = H_READ(s, mt);
#pragma unroll
          for (int e = 0; e < 4; ++e) { s1[mt] = dot2(v[e], 0x3f803f80u, s1[mt]); s2[mt] = dot2(v[e], v[e], s2[mt]); }
        }
#pragma unroll
      for (int mt = 0; mt < 4; ++mt) {
        const float mean = swap32_add(swap16_add(s1[mt])) * (1.f / 512.f);
        const float rstd = __builtin_amdgcn_rsqf(fmaxf(swap32_add(swap16_add(s2[mt])) * (1.f / 512.f) - mean * mean, 0.f) + 1e-5f);
        if (g4 == 0) *(f2*)(smem + L_ST + (16 * mt + c16) * 8) = f2{mean, rstd};
      }
      bar();
      W2_NEXT();
    } else {
      f4 acc[2][4] = {};
      u4 hb[2][4];
#pragma unroll
      for (int mt = 0; mt < 4; ++mt) hb[0][mt] = H_READ(0, mt);
#pragma unroll
      for (int s = 0; s < 16; ++s) {
        if (s < 15) {
#pragma unroll
          for (int mt = 0; mt < 4; ++mt) hb[(s + 1) & 1][mt] = H_READ(s + 1, mt);
        }
        SB();
#pragma unroll
        for (int mt = 0; mt < 4; ++mt) {
          acc[0][mt] = mfma16(a1f[s][0], hb[s & 1][mt], acc[0][mt]);
          acc[1][mt] = mfma16(a1f[s][1], hb[s & 1][mt], acc[1][mt]);
        }
        SB();
      }
      STAMP(14);
      bar();
      const char* cp = smem + L_CS + (w * 4 + g4) * 64;
      const f4 c0 = *(const f4*)cp, c1 = *(const f4*)(cp + 16), bb0 = *(const f4*)(cp + 32), bb1 = *(const f4*)(cp + 48);
      f2 stv[4];
#pragma unroll
      for (int mt = 0; mt < 4; ++mt) stv[mt] = *(const f2*)(smem + L_ST + (16 * mt + c16) * 8);
      W2_NEXT();
#pragma unroll
      for (int mt = 0; mt < 4; ++mt) {
        const f2 st = stv[mt];
        float y[8];
#pragma unroll
        for (int e = 0; e < 8; ++e) {
          const float cs = (e >> 2) ? c1[e & 3] : c0[e & 3];
          const float bb = (e >> 2) ? bb1[e & 3] : bb0[e & 3];
          const float v = fmaf(st[1], fmaf(-st[0], cs, acc[e >> 2][mt][e & 3]), bb);
          const float ex = __builtin_amdgcn_exp2f(v * LOG2E);
          const float r = __builtin_amdgcn_rcpf(fmaf(ex, ex + 2.f, 2.f));
          y[e] = fmaf(-2.f, v * r, v);
        }
        ST_SC1((u4{pk(y[0], y[1]), pk(y[2], y[3]), pk(y[4], y[5]), pk(y[6], y[7])}), WS_ACT + ((rb * 4 + mt) * 56 + 7 * j + w) * 1024, lane * 16);
      }
    }
  }
  drain();
  bar();
  if (tid == 0) put_flag(ws, WS_F4 + (rb * 8 + j) * 128, tag);
  STAMP(15);

  {
    W2_DMA(4, 131072 + w * 4096);
    wait_flags<PSL>(ws, WS_F4 + (rb * 8 + w) * 128, tag);
    STAMP(16);
    f4 acc[4][4] = {};
    u4 ab[7][4];
#define ACT_LOAD(s) _Pragma("unroll") for (int mt = 0; mt < 4; ++mt) ab[s][mt] = LD_SC1(WS_ACT + ((rb * 4 + mt) * 56 + 7 * w + (s)) * 1024, lane * 16)
    drain();
    ACT_LOAD(0); ACT_LOAD(1); ACT_LOAD(2); ACT_LOAD(3);
#pragma unroll
    for (int s = 0; s < 7; ++s) {
      SB();
      if (s >= 1 && s < 6) {
        const int base = (s == 1 ? L_H : s == 2 ? L_H + 4096 : s == 4 ? L_W2A : L_W2A + 4096) + w * 8192;
#pragma unroll
        for (int t = 0; t < 4; ++t) af[s + 1][t] = W2_FRAG(s == 3 ? 131072 + w * 4096 : base, t);
      }
      SB();
#pragma unroll
      for (int mt = 0; mt < 4; ++mt)
#pragma unroll
        for (int t = 0; t < 4; ++t) acc[t][mt] = mfma16(af[s][t], ab[s][mt], acc[t][mt]);
      SB();
      if (s + 4 < 7) { ACT_LOAD(s + 4); }
    }
    bar();
    STAMP(17);
    float y[8];
#pragma unroll
    for (int d = 0; d < 8; ++d) {
      const int mt = d >> 1, b = 2 * (d & 1);
      if (d != w) {
        char* rp = smem + L_RED2 + (d * 8 + w) * 2048 + lane * 16;
        *(f4*)rp = acc[b][mt];
        *(f4*)(rp + 1024) = acc[b + 1][mt];
      } else {
#pragma unroll
        for (int e = 0; e < 8; ++e) y[e] = acc[b + (e >> 2)][mt][e & 3];
      }
    }
    bar();
#pragma unroll
    for (int d = 1; d < 8; ++d) {
      const char* rp = smem + L_RED2 + (w * 8 + ((w + d) & 7)) * 2048 + lane * 16;
      f4 a = *(const f4*)rp, b = *(const f4*)(rp + 1024);
#pragma unroll
      for (int e = 0; e < 4; ++e) { y[e] += a[e]; y[4 + e] += b[e]; }
    }
    u4 r = {pk(y[0] + lo16(hr0[0]), y[1] + hi16(hr0[0])), pk(y[2] + lo16(hr0[1]), y[3] + hi16(hr0[1])),
            pk(y[4] + lo16(hr1[0]), y[5] + hi16(hr1[0])), pk(y[6] + lo16(hr1[1]), y[7] + hi16(hr1[1]))};
    *(u4*)(out + (size_t)(R0 + 16 * (w >> 1) + c16) * 1024 + (64 * j + 32 * (w & 1) + 8 * g4) * 2) = r;
  }
  if (bid == 0 && tid == 0) put_flag(ws, WS_E, tag);
  STAMP(18);
  }
}

extern "C" void kernel_launch(void* const* d_in, const int* in_sizes, int n_in, void* d_out, int out_size, void* d_ws,
                              size_t ws_size, hipStream_t stream) {
  typedef const unsigned char* cp;
  fused_block_kernel<<<256, 512, 0, stream>>>((cp)d_in[0], (cp)d_in[1], (cp)d_in[2], (cp)d_in[3], (cp)d_in[4], (cp)d_in[6],
                                              (cp)d_in[7], (cp)d_in[8], (unsigned char*)d_out, (unsigned char*)d_ws);
}
```

```cpp
#include <hip/hip_runtime.h>
#include <hip/hip_bf16.h>

typedef unsigned long long u64;
typedef __attribute__((ext_vector_type(8))) __bf16 bf8;
typedef __attribute__((ext_vector_type(2))) __bf16 bf2;
typedef __attribute__((ext_vector_type(4))) short s4;
typedef __attribute__((ext_vector_type(2))) float f2;
typedef __attribute__((ext_vector_type(4))) float f4;
typedef __attribute__((ext_vector_type(16))) float f16v;
typedef __attribute__((ext_vector_type(2))) unsigned u2;
typedef __attribute__((ext_vector_type(4))) unsigned u4;
#define AS1 __attribute__((address_space(1)))
#define AS3 __attribute__((address_space(3)))
#define DEVI __device__ __forceinline__
#define RLX_AGENT __ATOMIC_RELAXED, __HIP_MEMORY_SCOPE_AGENT

constexpr unsigned WS_E = 0, WS_F1 = 4096, WS_KMAX = 20480;
constexpr unsigned WS_F2 = 0x30000, WS_F3 = 0x38000, WS_F4 = 0x40000;
constexpr unsigned WS_K = 65536, WS_V = 131072, WS_O = 1u << 20, WS_H = 3u << 20, WS_ACT = 5u << 20, WS_END = 16u << 20;
constexpr int L_XN = 0, L_WQ = 65536, L_WKV = 131072, L_QX = 131072;
constexpr int L_K = 0, L_V = 65536, L_T1 = 131072;
constexpr int L_WO = 0, L_RED1 = 65536;
constexpr int L_H = 0, L_W2A = 65536, L_ST = 159744, L_CS = 160768;
constexpr int L_RED2 = 0;
constexpr int LDS_BYTES = 163840;
constexpr unsigned SPIN_CAP = 400000;
constexpr float LOG2E = 1.4426950408889634f;
constexpr float SAFE_BOUND = 50.0f;

#define PFW 0
#define ILV 1
#define RB16 1
#define CPF 1
#define OAUX 0
#define KVS 2
#define PSL 2
#define PSL1 4
#define STAMP(i) do {} while (0)

DEVI void bar() { asm volatile("s_waitcnt lgkmcnt(0)\n\ts_barrier" ::: "memory"); }
DEVI void drain() { asm volatile("s_waitcnt vmcnt(0)" ::: "memory"); }
DEVI unsigned pk(float a, float b) { f2 v = {a, b}; return __builtin_bit_cast(unsigned, __builtin_convertvector(v, bf2)); }
DEVI float lo16(unsigned v) { return __uint_as_float(v << 16); }
DEVI float hi16(unsigned v) { return __uint_as_float(v & 0xffff0000u); }
DEVI float dot2(unsigned a, unsigned b, float c) {
  return __builtin_amdgcn_fdot2_f32_bf16(__builtin_bit_cast(bf2, a), __builtin_bit_cast(bf2, b), c, false);
}
DEVI f16v mfma32(u4 a, u4 b, f16v c) {
  return __builtin_amdgcn_mfma_f32_32x32x16_bf16(__builtin_bit_cast(bf8, a), __builtin_bit_cast(bf8, b), c, 0, 0, 0);
}
DEVI f4 mfma16(u4 a, u4 b, f4 c) {
  return __builtin_amdgcn_mfma_f32_16x16x32_bf16(__builtin_bit_cast(bf8, a), __builtin_bit_cast(bf8, b), c, 0, 0, 0);
}
DEVI u4 lds16(const char* p) { return *(const u4*)p; }
DEVI u2 ldstr(const char* p) { return __builtin_bit_cast(u2, __builtin_amdgcn_ds_read_tr16_b64_v4i16((AS3 s4*)p)); }
DEVI u4 ldstr2(const char* p, int d) { u2 a = ldstr(p), b = ldstr(p + d); return u4{a[0], a[1], b[0], b[1]}; }
DEVI void dma16(const void* g, char* l) { __builtin_amdgcn_global_load_lds((const AS1 void*)g, (AS3 void*)l, 16, 0, 0); }
template <int C> DEVI float dpp(float v) { return __int_as_float(__builtin_amdgcn_update_dpp(0, __float_as_int(v), C, 0xf, 0xf, true)); }
DEVI float swap32_add(float v) { auto r = __builtin_amdgcn_permlane32_swap(__float_as_uint(v), __float_as_uint(v), false, false); return __uint_as_float(r[0]) + __uint_as_float(r[1]); }
DEVI float swap16_add(float v) { auto r = __builtin_amdgcn_permlane16_swap(__float_as_uint(v), __float_as_uint(v), false, false); return __uint_as_float(r[0]) + __uint_as_float(r[1]); }
DEVI float swap32_max(float v) { auto r = __builtin_amdgcn_permlane32_swap(__float_as_uint(v), __float_as_uint(v), false, false); return fmaxf(__uint_as_float(r[0]), __uint_as_float(r[1])); }
DEVI float swap16_max(float v) { auto r = __builtin_amdgcn_permlane16_swap(__float_as_uint(v), __float_as_uint(v), false, false); return fmaxf(__uint_as_float(r[0]), __uint_as_float(r[1])); }
DEVI float max16(float v) {
  v = fmaxf(v, dpp<0xB1>(v)); v = fmaxf(v, dpp<0x4E>(v)); v = fmaxf(v, dpp<0x141>(v)); return fmaxf(v, dpp<0x140>(v));
}
DEVI void put_flag(unsigned char* ws, unsigned off, u64 tag) { __hip_atomic_store((AS1 u64*)(ws + off), tag, RLX_AGENT); }
template <int SL> DEVI void wait_flags(unsigned char* ws, unsigned off, u64 tag) {
  for (unsigned spins = 0; spins < SPIN_CAP; ++spins) {
    if (__all(__hip_atomic_load((AS1 u64*)(ws + off), RLX_AGENT) == tag)) break;
    __builtin_amdgcn_s_sleep(SL);
  }
  __builtin_amdgcn_fence(__ATOMIC_ACQUIRE, "wavefront");
  asm volatile("" ::: "memory");
}
#define LD_SC1(so, vo) __builtin_amdgcn_raw_buffer_load_b128(rs, (vo), (so), 16)
#define LAUX 16
#define SAUX 16
#define DMA_SC1(so, vo, dst) __builtin_amdgcn_raw_ptr_buffer_load_lds(rs, (AS3 void*)(smem + (dst)), 16, (vo), (so), 0, 16)
#define LD_CL(so, vo) __builtin_amdgcn_raw_buffer_load_b128(rs, (vo), (so), LAUX)
#define ST_SC1(v, so, vo) __builtin_amdgcn_raw_buffer_store_b128((v), rs, (vo) + (so), 0, SAUX)

DEVI void pv_tile(const f16v& p, u4 v0, u4 v1, f16v& o) {
  u4 p0 = {pk(p[0], p[1]), pk(p[2], p[3]), pk(p[4], p[5]), pk(p[6], p[7])};
  o = mfma32(v0, p0, o);
  u4 p1 = {pk(p[8], p[9]), pk(p[10], p[11]), pk(p[12], p[13]), pk(p[14], p[15])};
  o = mfma32(v1, p1, o);
}
DEVI void tile_slow(const char* kp, const char* vp, bool vreal, unsigned vc, u4 q, float& m, f16v& o) {
  u4 v0 = lds16(vp), v1 = lds16(vp + 512);
#pragma unroll
  for (int e = 0; e < 4; ++e) { v0[e] = vreal ? v0[e] : vc; v1[e] = vreal ? v1[e] : vc; }
  f16v z = {0};
  f16v s = mfma32(lds16(kp), q, z);
  float t = s[0];
#pragma unroll
  for (int i = 1; i < 16; ++i) t = fmaxf(t, s[i]);
  float mn = fmaxf(m, swap32_max(t));
  float al = __builtin_amdgcn_exp2f(m - mn);
  m = mn;
  f16v p;
#pragma unroll
  for (int i = 0; i < 16; ++i) { p[i] = __builtin_amdgcn_exp2f(s[i] - mn); o[i] *= al; }
  pv_tile(p, v0, v1, o);
}

__global__ __launch_bounds__(512, 2) void fused_block_kernel(
    const unsigned char* __restrict__ x, const unsigned char* __restrict__ wq, const unsigned char* __restrict__ wk,
    const unsigned char* __restrict__ wv, const unsigned char* __restrict__ wo, const unsigned char* __restrict__ w1,
    const unsigned char* __restrict__ b1, const unsigned char* __restrict__ w2, unsigned char* __restrict__ out,
    unsigned char* ws) {
  __shared__ __attribute__((aligned(16))) char smem[LDS_BYTES];
  const int tid = threadIdx.x, lane = tid & 63;
  const int w = __builtin_amdgcn_readfirstlane(tid >> 6);
  const int bid = blockIdx.x;
  const int rb = (bid & 7) * 4 + ((bid >> 3) & 3), j = bid >> 5;
  const int R0 = rb * 64;
  const bool kvp = j == (rb & 7);
  const int c16 = lane & 15, g4 = lane >> 4, r32 = lane & 31, h2 = lane >> 5;
  const __amdgpu_buffer_rsrc_t rs = __builtin_amdgcn_make_buffer_rsrc((void*)ws, 0, (int)WS_END, 0x00020000);
  STAMP(0);
  const u64 e0 = __hip_atomic_load((AS1 u64*)(ws + WS_E), RLX_AGENT);

  unsigned cpf = 0;
  {
    const unsigned char* xr = x + (size_t)(R0 + 8 * w + (lane >> 3)) * 1024 + (lane & 7) * 16;
    u4 xv[8];
#pragma unroll
    for (int i = 0; i < 8; ++i) xv[i] = *(const u4*)(xr + i * 128);
    if (kvp) {
#pragma unroll
      for (int i = 0; i < 4; ++i)
        dma16(((i >> 1) ? wv : wk) + (size_t)((w * 2 + (i & 1)) * 64 + lane) * 16, smem + L_WKV + (i >> 1) * 16384 + (w * 2 + (i & 1)) * 1024);
    }
#pragma unroll
    for (int i = 0; i < 8; ++i) {
      const int k = i * 64 + w * 8 + (lane >> 3), pc = lane & 7;
      const int f = ((lane >> 4) & 1) | ((w & 1) << 1);
      dma16(wq + (size_t)k * 1024 + j * 128 + ((((pc >> 1) ^ f) << 1) | (pc & 1)) * 16, smem + L_WQ + (i * 8 + w) * 1024);
    }
    if (CPF) {
      u64 pc; unsigned len;
      asm volatile("s_getpc_b64 %0\n\ts_mov_b32 %1, .Lfused_block_tail - ." : "=s"(pc), "=s"(len));
      const unsigned co = (j * 64 + lane) * 128;
      if (w == 7 && co < len) asm volatile("global_load_dword %0, %1, %2" : "+v"(cpf) : "v"(co), "s"(pc) : "memory");
    }
    float s1 = 0.f, s2 = 0.f;
#pragma unroll
    for (int i = 0; i < 8; ++i)
#pragma unroll
      for (int e = 0; e < 4; ++e) { s1 = dot2(xv[i][e], 0x3f803f80u, s1); s2 = dot2(xv[i][e], xv[i][e], s2); }
    s1 += dpp<0xB1>(s1); s2 += dpp<0xB1>(s2);
    s1 += dpp<0x4E>(s1); s2 += dpp<0x4E>(s2);
    s1 += dpp<0x141>(s1); s2 += dpp<0x141>(s2);
    const float mean = s1 * (1.f / 512.f);
    const float rstd = __builtin_amdgcn_rsqf(fmaxf(s2 * (1.f / 512.f) - mean * mean, 0.f) + 1e-5f);
    const float nm = -mean * rstd;
    const int rl = 8 * w + (lane >> 3), q7 = lane & 7;
    char* xw = smem + L_XN + (((rl >> 4) * 16 + (q7 >> 2)) * 64 + (q7 & 3) * 16 + ((rl & 15) ^ q7)) * 16;
#pragma unroll
    for (int i = 0; i < 8; ++i) {
      u4 y;
#pragma unroll
      for (int e = 0; e < 4; ++e) y[e] = pk(fmaf(lo16(xv[i][e]), rstd, nm), fmaf(hi16(xv[i][e]), rstd, nm));
      *(u4*)(xw + i * 2048) = y;
    }
  }
  const u64 tag = (((u64)__builtin_amdgcn_readfirstlane((unsigned)(e0 >> 32)) << 32) | (unsigned)__builtin_amdgcn_readfirstlane((unsigned)e0)) + 1;
  STAMP(1);
  asm volatile("s_waitcnt vmcnt(8)" ::: "memory");
  bar();
  STAMP(2);
  const int xo0 = (g4 * 16 + (c16 ^ g4)) * 16, xo1 = (g4 * 16 + (c16 ^ (4 + g4))) * 16;
  if (kvp) {
    const char* wp = smem + L_WKV + (w & 1) * 16384 + (8 * g4 + (c16 >> 2)) * 32 + (c16 & 3) * 8;
    const char* xp = smem + L_XN + (w >> 1) * 16384;
    f4 a0 = {0}, a1 = {0};
    u4 wf[16], xf[16];
#pragma unroll
    for (int s = 0; s < 16; ++s) { wf[s] = ldstr2(wp + s * 1024, 128); xf[s] = lds16(xp + s * 1024 + ((s & 1) ? xo1 : xo0)); }
    __builtin_amdgcn_sched_barrier(0);
    if (w & 1) {
#pragma unroll
      for (int s = 0; s < 16; s += 2) { a0 = mfma16(xf[s], wf[s], a0); a1 = mfma16(xf[s + 1], wf[s + 1], a1); }
    } else {
#pragma unroll
      for (int s = 0; s < 16; s += 2) { a0 = mfma16(wf[s], xf[s], a0); a1 = mfma16(wf[s + 1], xf[s + 1], a1); }
    }
    a0 += a1;
    u2 kv = {pk(a0[0], a0[1]), pk(a0[2], a0[3])};
    const int key0 = R0 + 16 * (w >> 1);
    if (w & 1) {
      __builtin_amdgcn_raw_buffer_store_b64(kv, rs, WS_V + (key0 >> 4) * 512 + (g4 & 1) * 256 + c16 * 16 + (g4 >> 1) * 8, 0, 16);
      asm volatile("s_waitcnt vmcnt(1)" ::: "memory");
    } else {
      __builtin_amdgcn_raw_buffer_store_b64(kv, rs, WS_K + (key0 >> 5) * 1024 + ((g4 >> 1) * 32 + (key0 & 16) + c16) * 16 + (g4 & 1) * 8, 0, 16);
      float n2 = dot2(kv[0], kv[0], dot2(kv[1], kv[1], 0.f));
      n2 = max16(swap32_add(swap16_add(n2)));
      if (lane == 0) __builtin_amdgcn_raw_buffer_store_b32(__float_as_uint(n2), rs, WS_KMAX + (rb * 4 + (w >> 1)) * 4, 0, 16);
      asm volatile("s_waitcnt vmcnt(2)" ::: "memory");
    }
  } else {
    drain();
  }
  asm volatile("" :: "v"(cpf));
  bar();
  STAMP(3);
  const int rt = w & 1, ha = w >> 1;
  u4 qf;
  {
    const int f = ((c16 >> 3) & 1) | ((g4 & 1) << 1);
    const char* wp = smem + L_WQ + (8 * g4 + (c16 >> 2)) * 128 + ((ha ^ f) * 2) * 16 + (c16 & 3) * 8;
    const char* xp = smem + L_XN + rt * 32768;
    f4 a0 = {0}, a1 = {0};
    u4 wf[16], x0[16], x1[16];
#define Q_READ(s) do { wf[s] = ldstr2(wp + (s) * 4096, 512); x0[s] = lds16(xp + (s) * 1024 + (((s) & 1) ? xo1 : xo0)); \
                       x1[s] = lds16(xp + 16384 + (s) * 1024 + (((s) & 1) ? xo1 : xo0)); } while (0)
    Q_READ(0); Q_READ(1); Q_READ(2);
#pragma unroll
    for (int s = 0; s < 16; ++s) {
      __builtin_amdgcn_sched_barrier(0);
      if (s + 3 < 16) Q_READ(s + 3);
      __builtin_amdgcn_sched_barrier(0);
      a0 = mfma16(wf[s], x0[s], a0);
      a1 = mfma16(wf[s], x1[s], a1);
      if (s == KVS && kvp) {
        drain();
        bar();
        if (tid == 0) put_flag(ws, WS_F1 + rb * 128, tag);
      }
    }
    const float qs = 0.25f * LOG2E;
    auto r0 = __builtin_amdgcn_permlane16_swap(pk(a0[0] * qs, a0[1] * qs), pk(a1[0] * qs, a1[1] * qs), false, false);
    auto r1 = __builtin_amdgcn_permlane16_swap(pk(a0[2] * qs, a0[3] * qs), pk(a1[2] * qs, a1[3] * qs), false, false);
    qf = u4{r0[0], r1[0], r0[1], r1[1]};
    *(u4*)(smem + L_QX + w * 1024 + lane * 16) = qf;
  }
  STAMP(4);
  if (w == 0) wait_flags<PSL1>(ws, WS_F1 + r32 * 128, tag);
  if (PFW && w >= 4) {
    const unsigned char* pp = w1 + (size_t)(((bid >> 3) & 3) * 128 + ((tid & 255) >> 1)) * 3584 + 448 * j + (tid & 1) * 256;
    unsigned pa, pb;
    asm volatile("global_load_dword %0, %2, off\n\tglobal_load_dword %1, %2, off offset:128\n\ts_waitcnt vmcnt(0)" : "=&v"(pa), "=&v"(pb) : "v"(pp) : "memory");
  }
  bar();
  STAMP(5);

  u2 w1f[16][4];
  const __amdgpu_buffer_rsrc_t rw1 = __builtin_amdgcn_make_buffer_rsrc((void*)w1, 0, 512 * 3584, 0x00020000);
  const int w1o = w < 7 ? (lane >> 2) * 3584 + (224 * j + 32 * w) * 2 + ((lane & 3) ^ ((((lane >> 3) ^ (lane >> 4)) & 1) << 1)) * 16 : 0x40000000;
#define W1_DMA(i) __builtin_amdgcn_raw_ptr_buffer_load_lds(rw1, (AS3 void*)(smem + L_T1 + w * 4096 + ((i) & 3) * 1024), 16, w1o, 16 * (i) * 3584, 0, 0)
  const unsigned t0 = (unsigned)(size_t)(AS3 char*)(smem + L_T1 + w * 4096 + (16 * (g4 >> 1) + 4 * (g4 & 1) + (c16 >> 2)) * 64 +
                                                    ((((c16 >> 3) ^ g4) & 1) * 2 + ((c16 >> 1) & 1)) * 16 + (c16 & 1) * 8);
  const unsigned t1 = t0 ^ 32;
#define W1_READ(s, n) asm volatile("s_waitcnt vmcnt(%8)\n\tds_read_b64_tr_b16 %0, %4 offset:%6\n\tds_read_b64_tr_b16 %1, %4 offset:%7\n\t" \
    "ds_read_b64_tr_b16 %2, %5 offset:%6\n\tds_read_b64_tr_b16 %3, %5 offset:%7" \
    : "=&v"(w1f[s][0]), "=&v"(w1f[s][1]), "=&v"(w1f[s][2]), "=&v"(w1f[s][3]) : "v"(t0), "v"(t1), "i"(((s) & 1) * 2048), "i"(((s) & 1) * 2048 + 512), "i"(n))
  f16v o;
  u2 xres0, xres1;
  {
    const int sw = (w >> 2) * 32768 + (w & 3) * 1024, so = sw + lane * 16;
#define KV_LOAD(i) do { DMA_SC1(WS_K + (i) * 4096, so, L_K + sw + (i) * 4096); DMA_SC1(WS_V + (i) * 4096, so, L_V + sw + (i) * 4096); } while (0)
    const u4 qb = lds16(smem + L_QX + (w ^ 1) * 1024 + lane * 16);
    asm volatile("" ::: "memory");
    KV_LOAD(0);
    const u2 km = __builtin_bit_cast(u2, __builtin_amdgcn_raw_buffer_load_b64(rs, WS_KMAX + lane * 8, 0, 16));
    const float qn = fmaxf(swap32_add(dot2(qf[0], qf[0], dot2(qf[1], qf[1], dot2(qf[2], qf[2], dot2(qf[3], qf[3], 0.f))))),
                           swap32_add(dot2(qb[0], qb[0], dot2(qb[1], qb[1], dot2(qb[2], qb[2], dot2(qb[3], qb[3], 0.f))))));
    float kn = fmaxf(__uint_as_float(km[0]), __uint_as_float(km[1]));
    kn = swap32_max(swap16_max(max16(kn)));
    const bool safe = __all(qn * kn <= SAFE_BOUND * SAFE_BOUND);
    drain();
    bar();
    STAMP(6);
    const bool vreal = r32 < 16;
    const unsigned vc = (r32 == 16 || r32 == 20) ? 0x3f803f80u : 0u;
    const unsigned ka = (unsigned)(size_t)(AS3 char*)(smem + L_K + rt * 32768 + lane * 16);
    const unsigned va = (unsigned)(size_t)(AS3 char*)(smem + L_V + rt * 32768 + h2 * 256 + c16 * 16);
    const u64 vm = 0x0000ffff0000ffffull;
    u4 kf, v0 = {vc, vc, vc, vc}, v1 = v0, p0, p1 = {0, 0, 0, 0};
#define K_READ(u) asm volatile("ds_read_b128 %0, %1 offset:%2" : "=v"(kf) : "v"(ka), "i"((u) * 1024))
#define V_READ(v, u, hf) asm volatile("s_mov_b64 exec, %3\n\tds_read_b128 %0, %1 offset:%2\n\ts_mov_b64 exec, -1" : "+v"(v) : "v"(va), "i"((u) * 1024 + (hf) * 512), "s"(vm))
#define LGKM(n, v) asm volatile("s_waitcnt lgkmcnt(" #n ")")
#define SB() __builtin_amdgcn_sched_barrier(0)
#define EXPS(s, a, b) _Pragma("unroll") for (int i = a; i < b; ++i) asm volatile("v_exp_f32 %0, %0" : "+v"(s[i]))
#define PKS(s, b) u4{pk(s[b], s[b + 1]), pk(s[b + 2], s[b + 3]), pk(s[b + 4], s[b + 5]), pk(s[b + 6], s[b + 7])}
    const f16v zero = {0};
    f16v oa = zero, ob = zero, sa, sb;
    unsigned pf0 = 0, pf1 = 0;
#define PV(o, v, p) asm volatile("v_mfma_f32_32x32x16_bf16 %0, %1, %2, %0" : "+v"(o) : "v"(v), "v"(p))
#define QK(s, q) asm volatile("v_mfma_f32_32x32x16_bf16 %0, %1, %2, 0" : "=&v"(s) : "v"(kf), "v"(q))
    W1_DMA(0); W1_DMA(1);
    K_READ(0); V_READ(v0, 0, 0);
    LGKM(1, kf);
    QK(sa, qf);
    SB();
    asm volatile("s_nop 7\n\ts_nop 7\n\ts_nop 7" : "+v"(sa), "+v"(oa), "+v"(ob), "+v"(p1), "+v"(v1));
#pragma unroll
    for (int u = 0; u < 32; ++u) {
      SB();
      if (u == 3) asm volatile("s_waitcnt vmcnt(11)" ::: "memory");
      if (u == 8) asm volatile("s_waitcnt vmcnt(5)" ::: "memory");
      if (u == 3 || u == 8) bar();
      if (u < 4) { KV_LOAD(2 * u + 1); if (u < 3) KV_LOAD(2 * u + 2); }
      if (u < 30) W1_DMA(u + 2);
      SB();
      EXPS(sa, 0, 2); SB();
      PV(ob, v1, p1); SB();
      V_READ(v1, u, 1);
      EXPS(sa, 2, 6); SB();
      QK(sb, qb); SB();
      if (u < 31) K_READ(u + 1); else K_READ(u);
      if (u & 1) W1_READ(u >> 1, u == 1 ? 10 : u == 3 ? 8 : u == 11 ? 6 : u == 31 ? 0 : 2);
      EXPS(sa, 6, 8); SB();
      p0 = PKS(sa, 0); SB();
      EXPS(sa, 8, 10);
      if (u & 1) LGKM(6, v0); else LGKM(2, v0);
      SB();
      PV(oa, v0, p0); SB();
      EXPS(sa, 10, 16); SB();
      p1 = PKS(sa, 8); SB();
      if (u == 9) {
        const unsigned char* xr = x + (size_t)(R0 + 32 * rt + r32) * 1024 + (64 * j + 16 * ha + 4 * h2) * 2;
        xres0 = *(const u2*)xr; xres1 = *(const u2*)(xr + 16);
        const int cl = (bid >> 3) & 3;
        pf0 = *(const unsigned*)((w < 7 ? w2 + (size_t)(cl * 448 + w * 64 + lane) * 1024
                                        : wo + (size_t)(cl * 128 + lane) * 1024) + j * 128);
        pf1 = *(const unsigned*)(wo + (size_t)(cl * 128 + 64 + lane) * 1024 + j * 128);
      }
      if (u == 16) asm volatile("" :: "v"(pf0), "v"(pf1));

      SB();
      EXPS(sb, 0, 2);
      if (u & 1) LGKM(5, v1); else LGKM(1, v1);
      SB();
      PV(oa, v1, p1); SB();
      EXPS(sb, 2, 6);
      LGKM(0, kf);
      if (u & 1) asm volatile("" : "+v"(w1f[u >> 1][0]), "+v"(w1f[u >> 1][1]), "+v"(w1f[u >> 1][2]), "+v"(w1f[u >> 1][3]));
      SB();
      QK(sa, qf); SB();
      EXPS(sb, 6, 8); SB();
      p0 = PKS(sb, 0); SB();
      EXPS(sb, 8, 10); SB();
      PV(ob, v0, p0); SB();
      if (u < 31) V_READ(v0, u + 1, 0);
      EXPS(sb, 10, 16); SB();
      p1 = PKS(sb, 8);
    }
    asm volatile("s_nop 1");
    PV(ob, v1, p1);
    asm volatile("s_nop 7\n\ts_nop 7\n\ts_nop 7" : "+v"(oa), "+v"(ob));
    SB();
    char* xo = smem + L_T1 + w * 4096 + lane * 16;
    *(f4*)xo = f4{ob[0], ob[1], ob[2], ob[3]};
    *(f4*)(xo + 1024) = f4{ob[4], ob[5], ob[6], ob[7]};
    *(float*)(smem + L_T1 + w * 4096 + 2048 + lane * 4) = ob[8];
    if (!safe) {
      float m = -3.0e38f;
      oa = zero;
      const char* kp = smem + L_K + lane * 16;
      const char* vp = smem + L_V + h2 * 256 + c16 * 16;
#pragma unroll 1
      for (int t = 0; t < 64; ++t) tile_slow(kp + t * 1024, vp + t * 1024, vreal, vc, qf, m, oa);
    }
    bar();
    o = oa;
    if (safe) {
      const char* xi = smem + L_T1 + (w ^ 1) * 4096 + lane * 16;
      const f4 a = *(const f4*)xi, b = *(const f4*)(xi + 1024);
#pragma unroll
      for (int e = 0; e < 4; ++e) { o[e] += a[e]; o[4 + e] += b[e]; }
      o[8] += *(const float*)(smem + L_T1 + (w ^ 1) * 4096 + 2048 + lane * 4);
    }
  }
  STAMP(7);
  {
  int tid = threadIdx.x;
  asm volatile("" : "+v"(tid));
  const int lane = tid & 63, c16 = lane & 15, g4 = lane >> 4, h2 = lane >> 5;
  {
    const float il = __builtin_amdgcn_rcpf(o[8]);
    u4 of = {pk(o[0] * il, o[1] * il), pk(o[2] * il, o[3] * il), pk(o[4] * il, o[5] * il), pk(o[6] * il, o[7] * il)};
    ST_SC1(of, WS_O + ((rb * 2 + rt) * 32 + 4 * j + ha) * 1024, lane * 16);
  }
  asm volatile("" ::: "memory");
#pragma unroll
  for (int i = 0; i < 8; ++i) {
    const int k = i * 64 + w * 8 + (lane >> 3), pc = lane & 7;
    dma16(wo + (size_t)k * 1024 + j * 128 + (pc ^ (((lane >> 4) & 1) << 2)) * 16, smem + L_WO + (i * 8 + w) * 1024);
  }
  asm volatile("s_waitcnt vmcnt(8)" ::: "memory");
  bar();
  if (tid == 0) put_flag(ws, WS_F2 + (rb * 8 + j) * 128, tag);
  STAMP(8);

  u4 a1f[16][2];
  f4 cs0 = {0}, cs1 = {0};
  const u4 ones = {0x3f803f80u, 0x3f803f80u, 0x3f803f80u, 0x3f803f80u};
#pragma unroll
  for (int s = 0; s < 16; ++s) {
    a1f[s][0] = u4{w1f[s][0][0], w1f[s][0][1], w1f[s][1][0], w1f[s][1][1]};
    a1f[s][1] = u4{w1f[s][2][0], w1f[s][2][1], w1f[s][3][0], w1f[s][3][1]};
    cs0 = mfma16(a1f[s][0], ones, cs0); cs1 = mfma16(a1f[s][1], ones, cs1);
  }
  const unsigned char* bp = b1 + (224 * j + 32 * (w < 7 ? w : 6) + 4 * g4) * 2;
  const u2 ba = *(const u2*)bp, bb = *(const u2*)(bp + 32);
  if (c16 == 0) {
    char* cp = smem + L_CS + (w * 4 + g4) * 64;
    *(f4*)cp = cs0; *(f4*)(cp + 16) = cs1;
    *(f4*)(cp + 32) = f4{lo16(ba[0]), hi16(ba[0]), lo16(ba[1]), hi16(ba[1])};
    *(f4*)(cp + 48) = f4{lo16(bb[0]), hi16(bb[0]), lo16(bb[1]), hi16(bb[1])};
  }
  const int kq = ha;
  wait_flags<PSL>(ws, WS_F2 + (rb * 8 + 2 * kq + (lane & 1)) * 128, tag);
  STAMP(9);
  float hv[8];
  {
    u4 ob[8];
#pragma unroll
    for (int i = 0; i < 8; ++i) ob[i] = LD_CL(WS_O + ((rb * 2 + rt) * 32 + 8 * kq + i) * 1024, lane * 16);
    asm volatile("s_waitcnt vmcnt(8)" ::: "memory");
    bar();
    const int q = c16 >> 2, u = g4 & 1;
    const char* wp = smem + L_WO + (kq * 128 + 4 * h2 + q) * 128 + (c16 & 3) * 8;
    const int po0 = (u ^ ((q >> 1) << 1)) * 32, po1 = ((2 + u) ^ ((q >> 1) << 1)) * 32;
    f16v acc[2] = {{0}, {0}};
#pragma unroll
    for (int i = 0; i < 8; ++i) {
      acc[0] = mfma32(ldstr2(wp + i * 2048 + po0, 1024), ob[i], acc[0]);
      acc[1] = mfma32(ldstr2(wp + i * 2048 + po1, 1024), ob[i], acc[1]);
    }
    STAMP(10);
#pragma unroll
    for (int d = 0; d < 4; ++d) {
      if (d != kq) {
        char* rp = smem + L_RED1 + ((rt + 2 * d) * 4 + kq) * 2048 + lane * 16;
        const f16v& a = acc[d >> 1];
        const int b = 8 * (d & 1);
        if (RB16 > 1) { *(u4*)rp = u4{pk(a[b], a[b + 1]), pk(a[b + 2], a[b + 3]), pk(a[b + 4], a[b + 5]), pk(a[b + 6], a[b + 7])}; } else {
        *(f4*)rp = f4{a[b], a[b + 1], a[b + 2], a[b + 3]};
        *(f4*)(rp + 1024) = f4{a[b + 4], a[b + 5], a[b + 6], a[b + 7]}; }
      } else {
#pragma unroll
        for (int e = 0; e < 8; ++e) hv[e] = acc[d >> 1][8 * (d & 1) + e];
      }
    }
    bar();
#pragma unroll
    for (int d = 1; d < 4; ++d) {
      const char* rp = smem + L_RED1 + (w * 4 + ((kq + d) & 3)) * 2048 + lane * 16;
      if (RB16 > 1) { const u4 a = lds16(rp);
#pragma unroll
        for (int e = 0; e < 4; ++e) { hv[2 * e] += lo16(a[e]); hv[2 * e + 1] += hi16(a[e]); }
      } else {
      f4 a = *(const f4*)rp, b = *(const f4*)(rp + 1024);
#pragma unroll
      for (int e = 0; e < 4; ++e) { hv[e] += a[e]; hv[4 + e] += b[e]; } }
    }
    u4 hf = {pk(hv[0] + lo16(xres0[0]), hv[1] + hi16(xres0[0])), pk(hv[2] + lo16(xres0[1]), hv[3] + hi16(xres0[1])),
             pk(hv[4] + lo16(xres1[0]), hv[5] + hi16(xres1[0])), pk(hv[6] + lo16(xres1[1]), hv[7] + hi16(xres1[1]))};
    ST_SC1(hf, WS_H + ((rb * 2 + rt) * 32 + 4 * j + kq) * 1024, lane * 16);
  }
  drain();
  bar();
  if (tid == 0) put_flag(ws, WS_F3 + (rb * 8 + j) * 128, tag);
  STAMP(11);

  const unsigned char* w2p = w2 + (size_t)(224 * w + (lane >> 3)) * 1024 + j * 128 + ((lane & 7) ^ (((lane >> 4) & 1) << 2)) * 16;
#define W2_DMA(s, dst) _Pragma("unroll") for (int q = 0; q < 4; ++q) dma16(w2p + (size_t)(32 * (s) + 8 * q) * 1024, smem + (dst) + q * 1024)
  W2_DMA(0, L_W2A + w * 8192); W2_DMA(1, L_W2A + w * 8192 + 4096);
  wait_flags<PSL>(ws, WS_F3 + (rb * 8 + w) * 128, tag);
  STAMP(12);
  {
#pragma unroll
    for (int i = 0; i < 8; ++i)
      DMA_SC1(WS_H + ((rb * 2 + (i >> 2)) * 32 + 4 * w + (i & 3)) * 1024, lane * 16, L_H + ((i >> 2) * 32 + 4 * w + (i & 3)) * 1024);
    drain();
  }
  bar();
  STAMP(13);
  u4 af[7][4];
  const int ro = (4 * g4 + (c16 >> 2)) * 128, co0 = ((c16 & 3) ^ (((c16 >> 3) & 1) << 2)) * 16, co1 = co0 ^ 64;
#define W2_FRAG(base, t) ldstr2(smem + (base) + ro + (((t) >> 1) ? co1 : co0) + ((t) & 1) * 8, 2048)
#define W2_NEXT() do { \
    _Pragma("unroll") for (int t = 0; t < 4; ++t) { af[0][t] = W2_FRAG(L_W2A + w * 8192, t); af[1][t] = W2_FRAG(L_W2A + w * 8192 + 4096, t); } \
    asm volatile("s_waitcnt lgkmcnt(0)" ::: "memory"); } while (0)
#define W2_STEP(i) W2_DMA((i) < 2 ? (i) + 2 : (i) + 3, ((i) < 2 ? L_H : L_W2A) + w * 8192 + ((i) & 1) * 4096)
  u2 hr0, hr1;
  {
    const int mt = w >> 1;
    const char* hp = smem + L_H + ((mt >> 1) * 32 + 4 * j + 2 * (w & 1) + (g4 >> 1)) * 1024 + (16 * (mt & 1) + c16) * 16 + (g4 & 1) * 8;
    hr0 = *(const u2*)hp; hr1 = *(const u2*)(hp + 512);
  }
  {
    const char* hp = smem + L_H + (g4 >> 1) * 1024 + ((g4 & 1) * 32 + c16) * 16;
#define H_READ(s, mt) lds16(hp + ((mt) >> 1) * 32768 + (s) * 2048 + ((mt) & 1) * 256)
    if (w == 7) {
      float s1[4] = {}, s2[4] = {};
#pragma unroll 2
      for (int s = 0; s < 16; ++s)
#pragma unroll
        for (int mt = 0; mt < 4; ++mt) {
          const u4 v = H_READ(s, mt);
#pragma unroll
          for (int e = 0; e < 4; ++e) { s1[mt] = dot2(v[e], 0x3f803f80u, s1[mt]); s2[mt] = dot2(v[e], v[e], s2[mt]); }
        }
#pragma unroll
      for (int mt = 0; mt < 4; ++mt) {
        const float mean = swap32_add(swap16_add(s1[mt])) * (1.f / 512.f);
        const float rstd = __builtin_amdgcn_rsqf(fmaxf(swap32_add(swap16_add(s2[mt])) * (1.f / 512.f) - mean * mean, 0.f) + 1e-5f);
        if (g4 == 0) *(f2*)(smem + L_ST + (16 * mt + c16) * 8) = f2{mean, rstd};
      }
      bar();
      W2_NEXT();
      W2_STEP(0); W2_STEP(1); W2_STEP(2); W2_STEP(3);
    } else {
      f4 acc[2][4] = {};
      u4 hb[2][4];
#pragma unroll
      for (int mt = 0; mt < 4; ++mt) hb[0][mt] = H_READ(0, mt);
#pragma unroll
      for (int s = 0; s < 16; ++s) {
        if (s < 15) {
#pragma unroll
          for (int mt = 0; mt < 4; ++mt) hb[(s + 1) & 1][mt] = H_READ(s + 1, mt);
        }
        SB();
#pragma unroll
        for (int mt = 0; mt < 4; ++mt) {
          acc[0][mt] = mfma16(a1f[s][0], hb[s & 1][mt], acc[0][mt]);
          acc[1][mt] = mfma16(a1f[s][1], hb[s & 1][mt], acc[1][mt]);
        }
        SB();
      }
      STAMP(14);
      bar();
      const char* cp = smem + L_CS + (w * 4 + g4) * 64;
      const f4 c0 = *(const f4*)cp, c1 = *(const f4*)(cp + 16), bb0 = *(const f4*)(cp + 32), bb1 = *(const f4*)(cp + 48);
      f2 stv[4];
#pragma unroll
      for (int mt = 0; mt < 4; ++mt) stv[mt] = *(const f2*)(smem + L_ST + (16 * mt + c16) * 8);
      W2_NEXT();
#pragma unroll
      for (int mt = 0; mt < 4; ++mt) {
        const f2 st = stv[mt];
        if (ILV) { W2_STEP(mt); } else if (mt == 0) { W2_STEP(0); W2_STEP(1); W2_STEP(2); W2_STEP(3); }
        SB();
        float y[8];
#pragma unroll
        for (int e = 0; e < 8; ++e) {
          const float cs = (e >> 2) ? c1[e & 3] : c0[e & 3];
          const float bb = (e >> 2) ? bb1[e & 3] : bb0[e & 3];
          const float v = fmaf(st[1], fmaf(-st[0], cs, acc[e >> 2][mt][e & 3]), bb);
          const float ex = __builtin_amdgcn_exp2f(v * LOG2E);
          const float r = __builtin_amdgcn_rcpf(fmaf(ex, ex + 2.f, 2.f));
          y[e] = fmaf(-2.f, v * r, v);
        }
        ST_SC1((u4{pk(y[0], y[1]), pk(y[2], y[3]), pk(y[4], y[5]), pk(y[6], y[7])}), WS_ACT + ((rb * 4 + mt) * 56 + 7 * j + w) * 1024, lane * 16);
      }
    }
  }
  drain();
  bar();
  if (tid == 0) put_flag(ws, WS_F4 + (rb * 8 + j) * 128, tag);
  STAMP(15);

  {
    W2_DMA(4, 131072 + w * 4096);
    wait_flags<PSL>(ws, WS_F4 + (rb * 8 + w) * 128, tag);
    STAMP(16);
    f4 acc[4][4] = {};
    u4 ab[7][4];
#define ACT_LOAD(s) _Pragma("unroll") for (int mt = 0; mt < 4; ++mt) ab[s][mt] = LD_CL(WS_ACT + ((rb * 4 + mt) * 56 + 7 * w + (s)) * 1024, lane * 16)
    drain();
    ACT_LOAD(0); ACT_LOAD(1); ACT_LOAD(2); ACT_LOAD(3);
#pragma unroll
    for (int s = 0; s < 7; ++s) {
      SB();
      if (s >= 1 && s < 6) {
        const int base = (s == 1 ? L_H : s == 2 ? L_H + 4096 : s == 4 ? L_W2A : L_W2A + 4096) + w * 8192;
#pragma unroll
        for (int t = 0; t < 4; ++t) af[s + 1][t] = W2_FRAG(s == 3 ? 131072 + w * 4096 : base, t);
      }
      SB();
#pragma unroll
      for (int mt = 0; mt < 4; ++mt)
#pragma unroll
        for (int t = 0; t < 4; ++t) acc[t][mt] = mfma16(af[s][t], ab[s][mt], acc[t][mt]);
      SB();
      if (s + 4 < 7) { ACT_LOAD(s + 4); }
    }
    bar();
    STAMP(17);
    float y[8];
#pragma unroll
    for (int d = 0; d < 8; ++d) {
      const int mt = d >> 1, b = 2 * (d & 1);
      if (d != w) {
        char* rp = smem + L_RED2 + (d * 8 + w) * 2048 + lane * 16;
        const f4 a0 = acc[b][mt], a1 = acc[b + 1][mt];
        if (RB16) { *(u4*)rp = u4{pk(a0[0], a0[1]), pk(a0[2], a0[3]), pk(a1[0], a1[1]), pk(a1[2], a1[3])}; } else {
        *(f4*)rp = a0;
        *(f4*)(rp + 1024) = a1; }
      } else {
#pragma unroll
        for (int e = 0; e < 8; ++e) y[e] = acc[b + (e >> 2)][mt][e & 3];
      }
    }
    bar();
#pragma unroll
    for (int d = 1; d < 8; ++d) {
      const char* rp = smem + L_RED2 + (w * 8 + ((w + d) & 7)) * 2048 + lane * 16;
      if (RB16) { const u4 a = lds16(rp);
#pragma unroll
        for (int e = 0; e < 4; ++e) { y[2 * e] += lo16(a[e]); y[2 * e + 1] += hi16(a[e]); }
      } else {
      f4 a = *(const f4*)rp, b = *(const f4*)(rp + 1024);
#pragma unroll
      for (int e = 0; e < 4; ++e) { y[e] += a[e]; y[4 + e] += b[e]; } }
    }
    u4 r = {pk(y[0] + lo16(hr0[0]), y[1] + hi16(hr0[0])), pk(y[2] + lo16(hr0[1]), y[3] + hi16(hr0[1])),
            pk(y[4] + lo16(hr1[0]), y[5] + hi16(hr1[0])), pk(y[6] + lo16(hr1[1]), y[7] + hi16(hr1[1]))};
    const __amdgpu_buffer_rsrc_t ro_ = __builtin_amdgcn_make_buffer_rsrc((void*)out, 0, 2048 * 1024, 0x00020000);
    __builtin_amdgcn_raw_buffer_store_b128(r, ro_, (R0 + 16 * (w >> 1) + c16) * 1024 + (64 * j + 32 * (w & 1) + 8 * g4) * 2, 0, OAUX);
  }
  if (bid == 0 && tid == 0) put_flag(ws, WS_E, tag);
  STAMP(18);
  asm volatile(".Lfused_block_tail:");
  }
}

extern "C" void kernel_launch(void* const* d_in, const int* in_sizes, int n_in, void* d_out, int out_size, void* d_ws,
                              size_t ws_size, hipStream_t stream) {
  typedef const unsigned char* cp;
  fused_block_kernel<<<256, 512, 0, stream>>>((cp)d_in[0], (cp)d_in[1], (cp)d_in[2], (cp)d_in[3], (cp)d_in[4], (cp)d_in[6],
                                              (cp)d_in[7], (cp)d_in[8], (unsigned char*)d_out, (unsigned char*)d_ws);
}
```

```cpp
#include <hip/hip_runtime.h>
#include <hip/hip_bf16.h>

typedef unsigned long long u64;
typedef __attribute__((ext_vector_type(8))) __bf16 bf8;
typedef __attribute__((ext_vector_type(2))) __bf16 bf2;
typedef __attribute__((ext_vector_type(4))) short s4;
typedef __attribute__((ext_vector_type(2))) float f2;
typedef __attribute__((ext_vector_type(4))) float f4;
typedef __attribute__((ext_vector_type(16))) float f16v;
typedef __attribute__((ext_vector_type(2))) unsigned u2;
typedef __attribute__((ext_vector_type(4))) unsigned u4;
#define AS1 __attribute__((address_space(1)))
#define AS3 __attribute__((address_space(3)))
#define DEVI __device__ __forceinline__
#define RLX_AGENT __ATOMIC_RELAXED, __HIP_MEMORY_SCOPE_AGENT

constexpr unsigned WS_E = 0, WS_F1 = 4096, WS_KMAX = 20480;
constexpr unsigned WS_F2 = 0x30000, WS_F3 = 0x38000, WS_F4 = 0x40000;
constexpr unsigned WS_K = 65536, WS_V = 131072, WS_O = 1u << 20, WS_H = 3u << 20, WS_ACT = 5u << 20, WS_END = 16u << 20;
constexpr int L_XN = 0, L_WQ = 65536, L_WKV = 131072, L_QX = 131072;
constexpr int L_K = 0, L_V = 65536, L_T1 = 131072;
constexpr int L_WO = 0, L_RED1 = 65536;
constexpr int L_H = 0, L_W2A = 65536, L_ST = 159744, L_SF = 160256, L_CS = 160768;
constexpr int L_RED2 = 0;
constexpr int LDS_BYTES = 163840;
constexpr unsigned SPIN_CAP = 400000;
constexpr float LOG2E = 1.4426950408889634f;
constexpr float SAFE_BOUND = 50.0f;

#define PFW 0
#define PRI 0
#define RB16 1
#define CPF 1
#define OAUX 0
#define EARLY 1
#define SR 8
#define KVS 2
#define PSL 2
#define PSL1 4
#define STAMP(i) do {} while (0)

DEVI void bar() { asm volatile("s_waitcnt lgkmcnt(0)\n\ts_barrier" ::: "memory"); }
DEVI void drain() { asm volatile("s_waitcnt vmcnt(0)" ::: "memory"); }
DEVI unsigned pk(float a, float b) { f2 v = {a, b}; return __builtin_bit_cast(unsigned, __builtin_convertvector(v, bf2)); }
DEVI float lo16(unsigned v) { return __uint_as_float(v << 16); }
DEVI float hi16(unsigned v) { return __uint_as_float(v & 0xffff0000u); }
DEVI float dot2(unsigned a, unsigned b, float c) {
  return __builtin_amdgcn_fdot2_f32_bf16(__builtin_bit_cast(bf2, a), __builtin_bit_cast(bf2, b), c, false);
}
DEVI f16v mfma32(u4 a, u4 b, f16v c) {
  return __builtin_amdgcn_mfma_f32_32x32x16_bf16(__builtin_bit_cast(bf8, a), __builtin_bit_cast(bf8, b), c, 0, 0, 0);
}
DEVI f4 mfma16(u4 a, u4 b, f4 c) {
  return __builtin_amdgcn_mfma_f32_16x16x32_bf16(__builtin_bit_cast(bf8, a), __builtin_bit_cast(bf8, b), c, 0, 0, 0);
}
DEVI u4 lds16(const char* p) { return *(const u4*)p; }
DEVI u2 ldstr(const char* p) { return __builtin_bit_cast(u2, __builtin_amdgcn_ds_read_tr16_b64_v4i16((AS3 s4*)p)); }
DEVI u4 ldstr2(const char* p, int d) { u2 a = ldstr(p), b = ldstr(p + d); return u4{a[0], a[1], b[0], b[1]}; }
DEVI void dma16(const void* g, char* l) { __builtin_amdgcn_global_load_lds((const AS1 void*)g, (AS3 void*)l, 16, 0, 0); }
template <int C> DEVI float dpp(float v) { return __int_as_float(__builtin_amdgcn_update_dpp(0, __float_as_int(v), C, 0xf, 0xf, true)); }
DEVI float swap32_add(float v) { auto r = __builtin_amdgcn_permlane32_swap(__float_as_uint(v), __float_as_uint(v), false, false); return __uint_as_float(r[0]) + __uint_as_float(r[1]); }
DEVI float swap16_add(float v) { auto r = __builtin_amdgcn_permlane16_swap(__float_as_uint(v), __float_as_uint(v), false, false); return __uint_as_float(r[0]) + __uint_as_float(r[1]); }
DEVI float swap32_max(float v) { auto r = __builtin_amdgcn_permlane32_swap(__float_as_uint(v), __float_as_uint(v), false, false); return fmaxf(__uint_as_float(r[0]), __uint_as_float(r[1])); }
DEVI float swap16_max(float v) { auto r = __builtin_amdgcn_permlane16_swap(__float_as_uint(v), __float_as_uint(v), false, false); return fmaxf(__uint_as_float(r[0]), __uint_as_float(r[1])); }
DEVI float max16(float v) {
  v = fmaxf(v, dpp<0xB1>(v)); v = fmaxf(v, dpp<0x4E>(v)); v = fmaxf(v, dpp<0x141>(v)); return fmaxf(v, dpp<0x140>(v));
}
DEVI void put_flag(unsigned char* ws, unsigned off, u64 tag) { __hip_atomic_store((AS1 u64*)(ws + off), tag, RLX_AGENT); }
template <int SL> DEVI void wait_flags(unsigned char* ws, unsigned off, u64 tag) {
  for (unsigned spins = 0; spins < SPIN_CAP; ++spins) {
    if (__all(__hip_atomic_load((AS1 u64*)(ws + off), RLX_AGENT) == tag)) break;
    __builtin_amdgcn_s_sleep(SL);
  }
  __builtin_amdgcn_fence(__ATOMIC_ACQUIRE, "wavefront");
  asm volatile("" ::: "memory");
}
#define LD_SC1(so, vo) __builtin_amdgcn_raw_buffer_load_b128(rs, (vo), (so), 16)
#define LAUX 16
#define SAUX 16
#define DMA_SC1(so, vo, dst) __builtin_amdgcn_raw_ptr_buffer_load_lds(rs, (AS3 void*)(smem + (dst)), 16, (vo), (so), 0, 16)
#define LD_CL(so, vo) __builtin_amdgcn_raw_buffer_load_b128(rs, (vo), (so), LAUX)
#define ST_SC1(v, so, vo) __builtin_amdgcn_raw_buffer_store_b128((v), rs, (vo) + (so), 0, SAUX)

DEVI void pv_tile(const f16v& p, u4 v0, u4 v1, f16v& o) {
  u4 p0 = {pk(p[0], p[1]), pk(p[2], p[3]), pk(p[4], p[5]), pk(p[6], p[7])};
  o = mfma32(v0, p0, o);
  u4 p1 = {pk(p[8], p[9]), pk(p[10], p[11]), pk(p[12], p[13]), pk(p[14], p[15])};
  o = mfma32(v1, p1, o);
}
DEVI void tile_slow(const char* kp, const char* vp, bool vreal, unsigned vc, u4 q, float& m, f16v& o) {
  u4 v0 = lds16(vp), v1 = lds16(vp + 512);
#pragma unroll
  for (int e = 0; e < 4; ++e) { v0[e] = vreal ? v0[e] : vc; v1[e] = vreal ? v1[e] : vc; }
  f16v z = {0};
  f16v s = mfma32(lds16(kp), q, z);
  float t = s[0];
#pragma unroll
  for (int i = 1; i < 16; ++i) t = fmaxf(t, s[i]);
  float mn = fmaxf(m, swap32_max(t));
  float al = __builtin_amdgcn_exp2f(m - mn);
  m = mn;
  f16v p;
#pragma unroll
  for (int i = 0; i < 16; ++i) { p[i] = __builtin_amdgcn_exp2f(s[i] - mn); o[i] *= al; }
  pv_tile(p, v0, v1, o);
}

__global__ __launch_bounds__(512, 2) void fused_block_kernel(
    const unsigned char* __restrict__ x, const unsigned char* __restrict__ wq, const unsigned char* __restrict__ wk,
    const unsigned char* __restrict__ wv, const unsigned char* __restrict__ wo, const unsigned char* __restrict__ w1,
    const unsigned char* __restrict__ b1, const unsigned char* __restrict__ w2, unsigned char* __restrict__ out,
    unsigned char* ws) {
  __shared__ __attribute__((aligned(16))) char smem[LDS_BYTES];
  const int tid = threadIdx.x, lane = tid & 63;
  const int w = __builtin_amdgcn_readfirstlane(tid >> 6);
  const int bid = blockIdx.x;
  const int rb = (bid & 7) * 4 + ((bid >> 3) & 3), j = bid >> 5;
  const int R0 = rb * 64;
  const bool kvp = j == (rb & 7);
  const int c16 = lane & 15, g4 = lane >> 4, r32 = lane & 31, h2 = lane >> 5;
  const __amdgpu_buffer_rsrc_t rs = __builtin_amdgcn_make_buffer_rsrc((void*)ws, 0, (int)WS_END, 0x00020000);
  STAMP(0);
  const u64 e0 = __hip_atomic_load((AS1 u64*)(ws + WS_E), RLX_AGENT);

  unsigned cpf = 0;
  {
    const unsigned char* xr = x + (size_t)(R0 + 8 * w + (lane >> 3)) * 1024 + (lane & 7) * 16;
    u4 xv[8];
#pragma unroll
    for (int i = 0; i < 8; ++i) xv[i] = *(const u4*)(xr + i * 128);
    if (kvp) {
#pragma unroll
      for (int i = 0; i < 4; ++i)
        dma16(((i >> 1) ? wv : wk) + (size_t)((w * 2 + (i & 1)) * 64 + lane) * 16, smem + L_WKV + (i >> 1) * 16384 + (w * 2 + (i & 1)) * 1024);
    }
#pragma unroll
    for (int i = 0; i < 8; ++i) {
      const int k = i * 64 + w * 8 + (lane >> 3), pc = lane & 7;
      const int f = ((lane >> 4) & 1) | ((w & 1) << 1);
      dma16(wq + (size_t)k * 1024 + j * 128 + ((((pc >> 1) ^ f) << 1) | (pc & 1)) * 16, smem + L_WQ + (i * 8 + w) * 1024);
    }
    if (CPF) {
      u64 pc; unsigned len;
      asm volatile("s_getpc_b64 %0\n\ts_mov_b32 %1, .Lfused_block_tail - ." : "=s"(pc), "=s"(len));
      const unsigned co = (j * 64 + lane) * 128;
      if (w == 7 && co < len) asm volatile("global_load_dword %0, %1, %2" : "+v"(cpf) : "v"(co), "s"(pc) : "memory");
    }
    float s1 = 0.f, s2 = 0.f;
#pragma unroll
    for (int i = 0; i < 8; ++i)
#pragma unroll
      for (int e = 0; e < 4; ++e) { s1 = dot2(xv[i][e], 0x3f803f80u, s1); s2 = dot2(xv[i][e], xv[i][e], s2); }
    s1 += dpp<0xB1>(s1); s2 += dpp<0xB1>(s2);
    s1 += dpp<0x4E>(s1); s2 += dpp<0x4E>(s2);
    s1 += dpp<0x141>(s1); s2 += dpp<0x141>(s2);
    const float mean = s1 * (1.f / 512.f);
    const float rstd = __builtin_amdgcn_rsqf(fmaxf(s2 * (1.f / 512.f) - mean * mean, 0.f) + 1e-5f);
    const float nm = -mean * rstd;
    const int rl = 8 * w + (lane >> 3), q7 = lane & 7;
    char* xw = smem + L_XN + (((rl >> 4) * 16 + (q7 >> 2)) * 64 + (q7 & 3) * 16 + ((rl & 15) ^ q7)) * 16;
#pragma unroll
    for (int i = 0; i < 8; ++i) {
      u4 y;
#pragma unroll
      for (int e = 0; e < 4; ++e) y[e] = pk(fmaf(lo16(xv[i][e]), rstd, nm), fmaf(hi16(xv[i][e]), rstd, nm));
      *(u4*)(xw + i * 2048) = y;
    }
  }
  const u64 tag = (((u64)__builtin_amdgcn_readfirstlane((unsigned)(e0 >> 32)) << 32) | (unsigned)__builtin_amdgcn_readfirstlane((unsigned)e0)) + 1;
  STAMP(1);
  asm volatile("s_waitcnt vmcnt(8)" ::: "memory");
  bar();
  STAMP(2);
  const int xo0 = (g4 * 16 + (c16 ^ g4)) * 16, xo1 = (g4 * 16 + (c16 ^ (4 + g4))) * 16;
  if (kvp) {
    const char* wp = smem + L_WKV + (w & 1) * 16384 + (8 * g4 + (c16 >> 2)) * 32 + (c16 & 3) * 8;
    const char* xp = smem + L_XN + (w >> 1) * 16384;
    f4 a0 = {0}, a1 = {0};
    u4 wf[16], xf[16];
#pragma unroll
    for (int s = 0; s < 16; ++s) { wf[s] = ldstr2(wp + s * 1024, 128); xf[s] = lds16(xp + s * 1024 + ((s & 1) ? xo1 : xo0)); }
    __builtin_amdgcn_sched_barrier(0);
    if (w & 1) {
#pragma unroll
      for (int s = 0; s < 16; s += 2) { a0 = mfma16(xf[s], wf[s], a0); a1 = mfma16(xf[s + 1], wf[s + 1], a1); }
    } else {
#pragma unroll
      for (int s = 0; s < 16; s += 2) { a0 = mfma16(wf[s], xf[s], a0); a1 = mfma16(wf[s + 1], xf[s + 1], a1); }
    }
    a0 += a1;
    u2 kv = {pk(a0[0], a0[1]), pk(a0[2], a0[3])};
    const int key0 = R0 + 16 * (w >> 1);
    if (w & 1) {
      __builtin_amdgcn_raw_buffer_store_b64(kv, rs, WS_V + (key0 >> 4) * 512 + (g4 & 1) * 256 + c16 * 16 + (g4 >> 1) * 8, 0, 16);
      asm volatile("s_waitcnt vmcnt(1)" ::: "memory");
    } else {
      __builtin_amdgcn_raw_buffer_store_b64(kv, rs, WS_K + (key0 >> 5) * 1024 + ((g4 >> 1) * 32 + (key0 & 16) + c16) * 16 + (g4 & 1) * 8, 0, 16);
      float n2 = dot2(kv[0], kv[0], dot2(kv[1], kv[1], 0.f));
      n2 = max16(swap32_add(swap16_add(n2)));
      if (lane == 0) __builtin_amdgcn_raw_buffer_store_b32(__float_as_uint(n2), rs, WS_KMAX + (rb * 4 + (w >> 1)) * 4, 0, 16);
      asm volatile("s_waitcnt vmcnt(2)" ::: "memory");
    }
  } else {
    drain();
  }
  asm volatile("" :: "v"(cpf));
  bar();
  STAMP(3);
  const int rt = w & 1, ha = w >> 1;
  u4 qf;
  {
    const int f = ((c16 >> 3) & 1) | ((g4 & 1) << 1);
    const char* wp = smem + L_WQ + (8 * g4 + (c16 >> 2)) * 128 + ((ha ^ f) * 2) * 16 + (c16 & 3) * 8;
    const char* xp = smem + L_XN + rt * 32768;
    f4 a0 = {0}, a1 = {0};
    u4 wf[16], x0[16], x1[16];
#define Q_READ(s) do { wf[s] = ldstr2(wp + (s) * 4096, 512); x0[s] = lds16(xp + (s) * 1024 + (((s) & 1) ? xo1 : xo0)); \
                       x1[s] = lds16(xp + 16384 + (s) * 1024 + (((s) & 1) ? xo1 : xo0)); } while (0)
    Q_READ(0); Q_READ(1); Q_READ(2);
#pragma unroll
    for (int s = 0; s < 16; ++s) {
      __builtin_amdgcn_sched_barrier(0);
      if (s + 3 < 16) Q_READ(s + 3);
      __builtin_amdgcn_sched_barrier(0);
      a0 = mfma16(wf[s], x0[s], a0);
      a1 = mfma16(wf[s], x1[s], a1);
      if (s == KVS && kvp) {
        drain();
        bar();
        if (tid == 0) put_flag(ws, WS_F1 + rb * 128, tag);
      }
    }
    const float qs = 0.25f * LOG2E;
    auto r0 = __builtin_amdgcn_permlane16_swap(pk(a0[0] * qs, a0[1] * qs), pk(a1[0] * qs, a1[1] * qs), false, false);
    auto r1 = __builtin_amdgcn_permlane16_swap(pk(a0[2] * qs, a0[3] * qs), pk(a1[2] * qs, a1[3] * qs), false, false);
    qf = u4{r0[0], r1[0], r0[1], r1[1]};
    *(u4*)(smem + L_QX + w * 1024 + lane * 16) = qf;
  }
  STAMP(4);
  if (w == 0) wait_flags<PSL1>(ws, WS_F1 + r32 * 128, tag);
  if (PFW && w >= 4) {
    const unsigned char* pp = w1 + (size_t)(((bid >> 3) & 3) * 128 + ((tid & 255) >> 1)) * 3584 + 448 * j + (tid & 1) * 256;
    unsigned pa, pb;
    asm volatile("global_load_dword %0, %2, off\n\tglobal_load_dword %1, %2, off offset:128\n\ts_waitcnt vmcnt(0)" : "=&v"(pa), "=&v"(pb) : "v"(pp) : "memory");
  }
  bar();
  STAMP(5);

  u2 w1f[16][4];
  const __amdgpu_buffer_rsrc_t rw1 = __builtin_amdgcn_make_buffer_rsrc((void*)w1, 0, 512 * 3584, 0x00020000);
  const int w1o = w < 7 ? (lane >> 2) * 3584 + (224 * j + 32 * w) * 2 + ((lane & 3) ^ ((((lane >> 3) ^ (lane >> 4)) & 1) << 1)) * 16 : 0x40000000;
#define W1_DMA(i) __builtin_amdgcn_raw_ptr_buffer_load_lds(rw1, (AS3 void*)(smem + L_T1 + w * 4096 + ((i) & 3) * 1024), 16, w1o, 16 * (i) * 3584, 0, 0)
  const unsigned t0 = (unsigned)(size_t)(AS3 char*)(smem + L_T1 + w * 4096 + (16 * (g4 >> 1) + 4 * (g4 & 1) + (c16 >> 2)) * 64 +
                                                    ((((c16 >> 3) ^ g4) & 1) * 2 + ((c16 >> 1) & 1)) * 16 + (c16 & 1) * 8);
  const unsigned t1 = t0 ^ 32;
#define W1_READ(s, n) asm volatile("s_waitcnt vmcnt(%8)\n\tds_read_b64_tr_b16 %0, %4 offset:%6\n\tds_read_b64_tr_b16 %1, %4 offset:%7\n\t" \
    "ds_read_b64_tr_b16 %2, %5 offset:%6\n\tds_read_b64_tr_b16 %3, %5 offset:%7" \
    : "=&v"(w1f[s][0]), "=&v"(w1f[s][1]), "=&v"(w1f[s][2]), "=&v"(w1f[s][3]) : "v"(t0), "v"(t1), "i"(((s) & 1) * 2048), "i"(((s) & 1) * 2048 + 512), "i"(n))
  f16v o;
  u2 xres0, xres1;
  {
    const int sw = (w >> 2) * 32768 + (w & 3) * 1024, so = sw + lane * 16;
#define KV_LOAD(i) do { DMA_SC1(WS_K + (i) * 4096, so, L_K + sw + (i) * 4096); DMA_SC1(WS_V + (i) * 4096, so, L_V + sw + (i) * 4096); } while (0)
    const u4 qb = lds16(smem + L_QX + (w ^ 1) * 1024 + lane * 16);
    asm volatile("" ::: "memory");
    KV_LOAD(0);
    const u2 km = __builtin_bit_cast(u2, __builtin_amdgcn_raw_buffer_load_b64(rs, WS_KMAX + lane * 8, 0, 16));
    const float qn = fmaxf(swap32_add(dot2(qf[0], qf[0], dot2(qf[1], qf[1], dot2(qf[2], qf[2], dot2(qf[3], qf[3], 0.f))))),
                           swap32_add(dot2(qb[0], qb[0], dot2(qb[1], qb[1], dot2(qb[2], qb[2], dot2(qb[3], qb[3], 0.f))))));
    float kn = fmaxf(__uint_as_float(km[0]), __uint_as_float(km[1]));
    kn = swap32_max(swap16_max(max16(kn)));
    const bool safe = __all(qn * kn <= SAFE_BOUND * SAFE_BOUND);
    drain();
    bar();
    STAMP(6);
    const bool vreal = r32 < 16;
    const unsigned vc = (r32 == 16 || r32 == 20) ? 0x3f803f80u : 0u;
    const unsigned ka = (unsigned)(size_t)(AS3 char*)(smem + L_K + rt * 32768 + lane * 16);
    const unsigned va = (unsigned)(size_t)(AS3 char*)(smem + L_V + rt * 32768 + h2 * 256 + c16 * 16);
    const u64 vm = 0x0000ffff0000ffffull;
    u4 kf, v0 = {vc, vc, vc, vc}, v1 = v0, p0, p1 = {0, 0, 0, 0};
#define K_READ(u) asm volatile("ds_read_b128 %0, %1 offset:%2" : "=v"(kf) : "v"(ka), "i"((u) * 1024))
#define V_READ(v, u, hf) asm volatile("s_mov_b64 exec, %3\n\tds_read_b128 %0, %1 offset:%2\n\ts_mov_b64 exec, -1" : "+v"(v) : "v"(va), "i"((u) * 1024 + (hf) * 512), "s"(vm))
#define LGKM(n, v) asm volatile("s_waitcnt lgkmcnt(" #n ")")
#define SB() __builtin_amdgcn_sched_barrier(0)
#define EXPS(s, a, b) _Pragma("unroll") for (int i = a; i < b; ++i) asm volatile("v_exp_f32 %0, %0" : "+v"(s[i]))
#define PKS(s, b) u4{pk(s[b], s[b + 1]), pk(s[b + 2], s[b + 3]), pk(s[b + 4], s[b + 5]), pk(s[b + 6], s[b + 7])}
    const f16v zero = {0};
    f16v oa = zero, ob = zero, sa, sb;
    unsigned pf0 = 0;
    const unsigned pfo = (w * 8 + (lane & 7)) * 1024;
#define PV(o, v, p) asm volatile("v_mfma_f32_32x32x16_bf16 %0, %1, %2, %0" : "+v"(o) : "v"(v), "v"(p))
#define QK(s, q) asm volatile("v_mfma_f32_32x32x16_bf16 %0, %1, %2, 0" : "=&v"(s) : "v"(kf), "v"(q))
    W1_DMA(0); W1_DMA(1);
    K_READ(0); V_READ(v0, 0, 0);
    LGKM(1, kf);
    QK(sa, qf);
    SB();
    asm volatile("s_nop 7\n\ts_nop 7\n\ts_nop 7" : "+v"(sa), "+v"(oa), "+v"(ob), "+v"(p1), "+v"(v1));
#pragma unroll
    for (int u = 0; u < 32; ++u) {
      SB();
      if (u == 3) asm volatile("s_waitcnt vmcnt(11)" ::: "memory");
      if (u == 8) asm volatile("s_waitcnt vmcnt(7)" ::: "memory");
      if (u == 3 || u == 8) bar();
      if (u < 4) { KV_LOAD(2 * u + 1); if (u < 3) KV_LOAD(2 * u + 2); }
      if (u < 30) W1_DMA(u + 2);
      SB();
      EXPS(sa, 0, 2); SB();
      PV(ob, v1, p1); SB();
      V_READ(v1, u, 1);
      EXPS(sa, 2, 6); SB();
      QK(sb, qb); SB();
      if (u < 31) K_READ(u + 1); else K_READ(u);
      if (u & 1) W1_READ(u >> 1, u == 1 ? 10 : u == 3 ? 8 : u == 11 ? 5 : u == 31 ? 0 : (u >= 7 && u <= 23) ? 3 : 2);
      EXPS(sa, 6, 8); SB();
      p0 = PKS(sa, 0); SB();
      EXPS(sa, 8, 10);
      if (u & 1) LGKM(6, v0); else LGKM(2, v0);
      SB();
      PV(oa, v0, p0); SB();
      EXPS(sa, 10, 16); SB();
      p1 = PKS(sa, 8); SB();
      if ((u & 1) && u >= 5 && u <= 21) {
        const int ix = (u - 5) >> 1, cl = (bid >> 3) & 3;
        const unsigned char* pb = (ix < 7 ? w2 + (size_t)(cl * 448 + ix * 64) * 1024 : wo + (size_t)(cl * 128 + (ix - 7) * 64) * 1024) + j * 128;
        asm volatile("s_mov_b64 exec, 0xff\n\tglobal_load_dword %0, %1, %2\n\ts_mov_b64 exec, -1" : "+v"(pf0) : "v"(pfo), "s"(pb));
      }
      if (u == 9) {
        const unsigned char* xr = x + (size_t)(R0 + 32 * rt + r32) * 1024 + (64 * j + 16 * ha + 4 * h2) * 2;
        xres0 = *(const u2*)xr; xres1 = *(const u2*)(xr + 16);
      }
      if (u == 31) asm volatile("" :: "v"(pf0));

      SB();
      EXPS(sb, 0, 2);
      if (u & 1) LGKM(5, v1); else LGKM(1, v1);
      SB();
      PV(oa, v1, p1); SB();
      EXPS(sb, 2, 6);
      LGKM(0, kf);
      if (u & 1) asm volatile("" : "+v"(w1f[u >> 1][0]), "+v"(w1f[u >> 1][1]), "+v"(w1f[u >> 1][2]), "+v"(w1f[u >> 1][3]));
      SB();
      QK(sa, qf); SB();
      EXPS(sb, 6, 8); SB();
      p0 = PKS(sb, 0); SB();
      EXPS(sb, 8, 10); SB();
      PV(ob, v0, p0); SB();
      if (u < 31) V_READ(v0, u + 1, 0);
      EXPS(sb, 10, 16); SB();
      p1 = PKS(sb, 8);
    }
    asm volatile("s_nop 1");
    PV(ob, v1, p1);
    asm volatile("s_nop 7\n\ts_nop 7\n\ts_nop 7" : "+v"(oa), "+v"(ob));
    SB();
    char* xo = smem + L_T1 + w * 4096 + lane * 16;
    *(f4*)xo = f4{ob[0], ob[1], ob[2], ob[3]};
    *(f4*)(xo + 1024) = f4{ob[4], ob[5], ob[6], ob[7]};
    *(float*)(smem + L_T1 + w * 4096 + 2048 + lane * 4) = ob[8];
    if (!safe) {
      float m = -3.0e38f;
      oa = zero;
      const char* kp = smem + L_K + lane * 16;
      const char* vp = smem + L_V + h2 * 256 + c16 * 16;
#pragma unroll 1
      for (int t = 0; t < 64; ++t) tile_slow(kp + t * 1024, vp + t * 1024, vreal, vc, qf, m, oa);
    }
    bar();
    o = oa;
    if (safe) {
      const char* xi = smem + L_T1 + (w ^ 1) * 4096 + lane * 16;
      const f4 a = *(const f4*)xi, b = *(const f4*)(xi + 1024);
#pragma unroll
      for (int e = 0; e < 4; ++e) { o[e] += a[e]; o[4 + e] += b[e]; }
      o[8] += *(const float*)(smem + L_T1 + (w ^ 1) * 4096 + 2048 + lane * 4);
    }
  }
  STAMP(7);
  {
  int tid = threadIdx.x;
  asm volatile("" : "+v"(tid));
  const int lane = tid & 63, c16 = lane & 15, g4 = lane >> 4, h2 = lane >> 5;
  {
    const float il = __builtin_amdgcn_rcpf(o[8]);
    u4 of = {pk(o[0] * il, o[1] * il), pk(o[2] * il, o[3] * il), pk(o[4] * il, o[5] * il), pk(o[6] * il, o[7] * il)};
    ST_SC1(of, WS_O + ((rb * 2 + rt) * 32 + 4 * j + ha) * 1024, lane * 16);
  }
  asm volatile("" ::: "memory");
#pragma unroll
  for (int i = 0; i < 8; ++i) {
    const int k = i * 64 + w * 8 + (lane >> 3), pc = lane & 7;
    dma16(wo + (size_t)k * 1024 + j * 128 + (pc ^ (((lane >> 4) & 1) << 2)) * 16, smem + L_WO + (i * 8 + w) * 1024);
  }
  asm volatile("s_waitcnt vmcnt(8)" ::: "memory");
  bar();
  if (tid == 0) put_flag(ws, WS_F2 + (rb * 8 + j) * 128, tag);
  STAMP(8);

  u4 a1f[16][2];
  f4 cs0 = {0}, cs1 = {0};
  const u4 ones = {0x3f803f80u, 0x3f803f80u, 0x3f803f80u, 0x3f803f80u};
#pragma unroll
  for (int s = 0; s < 16; ++s) {
    a1f[s][0] = u4{w1f[s][0][0], w1f[s][0][1], w1f[s][1][0], w1f[s][1][1]};
    a1f[s][1] = u4{w1f[s][2][0], w1f[s][2][1], w1f[s][3][0], w1f[s][3][1]};
    cs0 = mfma16(a1f[s][0], ones, cs0); cs1 = mfma16(a1f[s][1], ones, cs1);
  }
  const unsigned char* bp = b1 + (224 * j + 32 * (w < 7 ? w : 6) + 4 * g4) * 2;
  const u2 ba = *(const u2*)bp, bb = *(const u2*)(bp + 32);
  if (tid == 0) *(volatile unsigned*)(smem + L_SF) = 0;
  if (c16 == 0) {
    char* cp = smem + L_CS + (w * 4 + g4) * 64;
    *(f4*)cp = cs0; *(f4*)(cp + 16) = cs1;
    *(f4*)(cp + 32) = f4{lo16(ba[0]), hi16(ba[0]), lo16(ba[1]), hi16(ba[1])};
    *(f4*)(cp + 48) = f4{lo16(bb[0]), hi16(bb[0]), lo16(bb[1]), hi16(bb[1])};
  }
  const unsigned char* w2p = w2 + (size_t)(224 * w) * 1024 + j * 128;
  const unsigned w2v = (lane >> 3) * 1024 + ((lane & 7) ^ (((lane >> 4) & 1) << 2)) * 16;
  const unsigned lds0 = (unsigned)(size_t)(AS3 char*)smem;
#define W2_DMA(s, dst) _Pragma("unroll") for (int q = 0; q < 4; ++q) \
    asm volatile("s_mov_b32 m0, %2\n\ts_nop 0\n\tglobal_load_lds_dwordx4 %0, %1" \
                 :: "v"(w2v), "s"(w2p + (size_t)(32 * (s) + 8 * q) * 1024), "s"(lds0 + (dst) + q * 1024) : "memory", "m0")
  const int kq = ha;
  wait_flags<PSL>(ws, WS_F2 + (rb * 8 + 2 * kq + (lane & 1)) * 128, tag);
  STAMP(9);
  float hv[8];
  {
    u4 ob[8];
#pragma unroll
    for (int i = 0; i < 8; ++i) ob[i] = LD_CL(WS_O + ((rb * 2 + rt) * 32 + 8 * kq + i) * 1024, lane * 16);
    asm volatile("s_waitcnt vmcnt(8)" ::: "memory");
    bar();
    const int q = c16 >> 2, u = g4 & 1;
    const char* wp = smem + L_WO + (kq * 128 + 4 * h2 + q) * 128 + (c16 & 3) * 8;
    const int po0 = (u ^ ((q >> 1) << 1)) * 32, po1 = ((2 + u) ^ ((q >> 1) << 1)) * 32;
    f16v acc[2] = {{0}, {0}};
#pragma unroll
    for (int i = 0; i < 8; ++i) {
      acc[0] = mfma32(ldstr2(wp + i * 2048 + po0, 1024), ob[i], acc[0]);
      acc[1] = mfma32(ldstr2(wp + i * 2048 + po1, 1024), ob[i], acc[1]);
    }
    STAMP(10);
    if (w < 7) { W2_DMA(4, L_T1 + w * 4096); }
#pragma unroll
    for (int d = 0; d < 4; ++d) {
      if (d != kq) {
        char* rp = smem + L_RED1 + ((rt + 2 * d) * 4 + kq) * 2048 + lane * 16;
        const f16v& a = acc[d >> 1];
        const int b = 8 * (d & 1);
        if (RB16 > 1) { *(u4*)rp = u4{pk(a[b], a[b + 1]), pk(a[b + 2], a[b + 3]), pk(a[b + 4], a[b + 5]), pk(a[b + 6], a[b + 7])}; } else {
        *(f4*)rp = f4{a[b], a[b + 1], a[b + 2], a[b + 3]};
        *(f4*)(rp + 1024) = f4{a[b + 4], a[b + 5], a[b + 6], a[b + 7]}; }
      } else {
#pragma unroll
        for (int e = 0; e < 8; ++e) hv[e] = acc[d >> 1][8 * (d & 1) + e];
      }
    }
    bar();
#pragma unroll
    for (int d = 1; d < 4; ++d) {
      const char* rp = smem + L_RED1 + (w * 4 + ((kq + d) & 3)) * 2048 + lane * 16;
      if (RB16 > 1) { const u4 a = lds16(rp);
#pragma unroll
        for (int e = 0; e < 4; ++e) { hv[2 * e] += lo16(a[e]); hv[2 * e + 1] += hi16(a[e]); }
      } else {
      f4 a = *(const f4*)rp, b = *(const f4*)(rp + 1024);
#pragma unroll
      for (int e = 0; e < 4; ++e) { hv[e] += a[e]; hv[4 + e] += b[e]; } }
    }
    u4 hf = {pk(hv[0] + lo16(xres0[0]), hv[1] + hi16(xres0[0])), pk(hv[2] + lo16(xres0[1]), hv[3] + hi16(xres0[1])),
             pk(hv[4] + lo16(xres1[0]), hv[5] + hi16(xres1[0])), pk(hv[6] + lo16(xres1[1]), hv[7] + hi16(xres1[1]))};
    ST_SC1(hf, WS_H + ((rb * 2 + rt) * 32 + 4 * j + kq) * 1024, lane * 16);
  }
  drain();
  bar();
  if (tid == 0) put_flag(ws, WS_F3 + (rb * 8 + j) * 128, tag);
  STAMP(11);

  if (EARLY > 0) { W2_DMA(0, L_W2A + w * 8192); }
  if (EARLY > 1) { W2_DMA(1, L_W2A + w * 8192 + 4096); }
  wait_flags<PSL>(ws, WS_F3 + (rb * 8 + w) * 128, tag);
  STAMP(12);
  {
#pragma unroll
    for (int i = 0; i < 8; ++i)
      DMA_SC1(WS_H + ((rb * 2 + (i >> 2)) * 32 + 4 * w + (i & 3)) * 1024, lane * 16, L_H + ((i >> 2) * 32 + 4 * w + (i & 3)) * 1024);
    drain();
  }
  bar();
  STAMP(13);
  u4 af[7][4];
  const int ro = (4 * g4 + (c16 >> 2)) * 128, co0 = ((c16 & 3) ^ (((c16 >> 3) & 1) << 2)) * 16, co1 = co0 ^ 64;
#define W2_FRAG(base, t) ldstr2(smem + (base) + ro + (((t) >> 1) ? co1 : co0) + ((t) & 1) * 8, 2048)
#define W2_REGS() drain(); _Pragma("unroll") for (int t = 0; t < 4; ++t) { af[0][t] = W2_FRAG(L_W2A + w * 8192, t); af[1][t] = W2_FRAG(L_W2A + w * 8192 + 4096, t); }
#define W2_NEXT() do { asm volatile("s_waitcnt lgkmcnt(0)" ::: "memory"); W2_DMA(5, L_W2A + w * 8192); W2_DMA(6, L_W2A + w * 8192 + 4096); } while (0)
  u2 hr0, hr1;
  {
    const int mt = w >> 1;
    const char* hp = smem + L_H + ((mt >> 1) * 32 + 4 * j + 2 * (w & 1) + (g4 >> 1)) * 1024 + (16 * (mt & 1) + c16) * 16 + (g4 & 1) * 8;
    hr0 = *(const u2*)hp; hr1 = *(const u2*)(hp + 512);
  }
  {
    const char* hp = smem + L_H + (g4 >> 1) * 1024 + ((g4 & 1) * 32 + c16) * 16;
#define H_READ(s, mt) lds16(hp + ((mt) >> 1) * 32768 + (s) * 2048 + ((mt) & 1) * 256)
    if (w == 7) {
      f4 s1[4] = {}, s2[4] = {};
      u4 hb[2][4];
      __builtin_amdgcn_s_setprio(2);
#pragma unroll
      for (int mt = 0; mt < 4; ++mt) hb[0][mt] = H_READ(0, mt);
#pragma unroll
      for (int s = 0; s < 16; ++s) {
        if (s < 15) {
#pragma unroll
          for (int mt = 0; mt < 4; ++mt) hb[(s + 1) & 1][mt] = H_READ(s + 1, mt);
        }
        if (s == 0 && EARLY < 1) { W2_DMA(0, L_W2A + w * 8192); }
        if (s == 2 && EARLY < 2) { W2_DMA(1, L_W2A + w * 8192 + 4096); }
        SB();
#pragma unroll
        for (int mt = 0; mt < 4; ++mt) { s1[mt] = mfma16(ones, hb[s & 1][mt], s1[mt]); s2[mt] = mfma16(hb[s & 1][mt], hb[s & 1][mt], s2[mt]); }
        SB();
      }
#pragma unroll
      for (int mt = 0; mt < 4; ++mt) {
        const float mean = s1[mt][0] * (1.f / 512.f);
        const float sq = (c16 & 2) ? ((c16 & 1) ? s2[mt][3] : s2[mt][2]) : ((c16 & 1) ? s2[mt][1] : s2[mt][0]);
        const float rstd = __builtin_amdgcn_rsqf(fmaxf(sq * (1.f / 512.f) - mean * mean, 0.f) + 1e-5f);
        if (g4 == (c16 >> 2)) *(f2*)(smem + L_ST + (16 * mt + c16) * 8) = f2{mean, rstd};
      }
      asm volatile("s_waitcnt lgkmcnt(0)" ::: "memory");
      if (lane == 0) *(volatile unsigned*)(smem + L_SF) = 1;
      W2_REGS();
      W2_NEXT();
    } else {
      f4 acc[2][4] = {};
      u4 hb[2][4];
      if (PRI && w < 3) __builtin_amdgcn_s_setprio(1);
#pragma unroll
      for (int mt = 0; mt < 4; ++mt) hb[0][mt] = H_READ(0, mt);
#pragma unroll
      for (int s = 0; s < 16; ++s) {
        if (s < 15) {
#pragma unroll
          for (int mt = 0; mt < 4; ++mt) hb[(s + 1) & 1][mt] = H_READ(s + 1, mt);
        }
        if (s == 0 && EARLY < 1) { W2_DMA(0, L_W2A + w * 8192); }
        if (s == 2 && EARLY < 2) { W2_DMA(1, L_W2A + w * 8192 + 4096); }
        if (s == SR) { W2_REGS(); }
        if (s == SR + 2) { asm volatile("s_waitcnt lgkmcnt(0)" ::: "memory"); W2_DMA(5, L_W2A + w * 8192); }
        if (s == SR + 4) { W2_DMA(6, L_W2A + w * 8192 + 4096); }
        SB();
#pragma unroll
        for (int mt = 0; mt < 4; ++mt) {
          acc[0][mt] = mfma16(a1f[s][0], hb[s & 1][mt], acc[0][mt]);
          acc[1][mt] = mfma16(a1f[s][1], hb[s & 1][mt], acc[1][mt]);
        }
        SB();
      }
      STAMP(14);
      while (*(volatile unsigned*)(smem + L_SF) == 0) __builtin_amdgcn_s_sleep(1);
      asm volatile("" ::: "memory");
      const char* cp = smem + L_CS + (w * 4 + g4) * 64;
      const f4 c0 = *(const f4*)cp, c1 = *(const f4*)(cp + 16), bb0 = *(const f4*)(cp + 32), bb1 = *(const f4*)(cp + 48);
      f2 stv[4];
#pragma unroll
      for (int mt = 0; mt < 4; ++mt) stv[mt] = *(const f2*)(smem + L_ST + (16 * mt + c16) * 8);
#pragma unroll
      for (int mt = 0; mt < 4; ++mt) {
        const f2 st = stv[mt];
        float y[8];
#pragma unroll
        for (int e = 0; e < 8; ++e) {
          const float cs = (e >> 2) ? c1[e & 3] : c0[e & 3];
          const float bb = (e >> 2) ? bb1[e & 3] : bb0[e & 3];
          const float v = fmaf(st[1], fmaf(-st[0], cs, acc[e >> 2][mt][e & 3]), bb);
          const float ex = __builtin_amdgcn_exp2f(v * LOG2E);
          const float r = __builtin_amdgcn_rcpf(fmaf(ex, ex + 2.f, 2.f));
          y[e] = fmaf(-2.f, v * r, v);
        }
        ST_SC1((u4{pk(y[0], y[1]), pk(y[2], y[3]), pk(y[4], y[5]), pk(y[6], y[7])}), WS_ACT + ((rb * 4 + mt) * 56 + 7 * j + w) * 1024, lane * 16);
      }
    }
  }
  drain();
  bar();
  __builtin_amdgcn_s_setprio(0);
  if (tid == 0) put_flag(ws, WS_F4 + (rb * 8 + j) * 128, tag);
  STAMP(15);

  {
    W2_DMA(2, L_H + w * 8192); W2_DMA(3, L_H + w * 8192 + 4096);
    if (w == 7) { W2_DMA(4, L_T1 + w * 4096); }
    wait_flags<PSL>(ws, WS_F4 + (rb * 8 + w) * 128, tag);
    STAMP(16);
    f4 acc[4][4] = {};
    u4 ab[7][4];
#define ACT_LOAD(s) _Pragma("unroll") for (int mt = 0; mt < 4; ++mt) ab[s][mt] = LD_CL(WS_ACT + ((rb * 4 + mt) * 56 + 7 * w + (s)) * 1024, lane * 16)
    drain();
    ACT_LOAD(0); ACT_LOAD(1); ACT_LOAD(2); ACT_LOAD(3);
#pragma unroll
    for (int s = 0; s < 7; ++s) {
      SB();
      if (s >= 1 && s < 6) {
        const int base = (s == 1 ? L_H : s == 2 ? L_H + 4096 : s == 4 ? L_W2A : L_W2A + 4096) + w * 8192;
#pragma unroll
        for (int t = 0; t < 4; ++t) af[s + 1][t] = W2_FRAG(s == 3 ? L_T1 + w * 4096 : base, t);
      }
      SB();
#pragma unroll
      for (int mt = 0; mt < 4; ++mt)
#pragma unroll
        for (int t = 0; t < 4; ++t) acc[t][mt] = mfma16(af[s][t], ab[s][mt], acc[t][mt]);
      SB();
      if (s + 4 < 7) { ACT_LOAD(s + 4); }
    }
    STAMP(17);
    float y[8];
#pragma unroll
    for (int d = 0; d < 8; ++d) {
      const int mt = d >> 1, b = 2 * (d & 1);
      if (d != w) {
        const f4 a0 = acc[b][mt], a1 = acc[b + 1][mt];
        *(u4*)(smem + L_RED2 + (w * 8 + d) * 1024 + lane * 16) = u4{pk(a0[0], a0[1]), pk(a0[2], a0[3]), pk(a1[0], a1[1]), pk(a1[2], a1[3])};
      } else {
#pragma unroll
        for (int e = 0; e < 8; ++e) y[e] = acc[b + (e >> 2)][mt][e & 3];
      }
    }
    bar();
#pragma unroll
    for (int d = 1; d < 8; ++d) {
      const u4 a = lds16(smem + L_RED2 + (((w + d) & 7) * 8 + w) * 1024 + lane * 16);
#pragma unroll
      for (int e = 0; e < 4; ++e) { y[2 * e] += lo16(a[e]); y[2 * e + 1] += hi16(a[e]); }
    }
    u4 r = {pk(y[0] + lo16(hr0[0]), y[1] + hi16(hr0[0])), pk(y[2] + lo16(hr0[1]), y[3] + hi16(hr0[1])),
            pk(y[4] + lo16(hr1[0]), y[5] + hi16(hr1[0])), pk(y[6] + lo16(hr1[1]), y[7] + hi16(hr1[1]))};
    const __amdgpu_buffer_rsrc_t ro_ = __builtin_amdgcn_make_buffer_rsrc((void*)out, 0, 2048 * 1024, 0x00020000);
    __builtin_amdgcn_raw_buffer_store_b128(r, ro_, (R0 + 16 * (w >> 1) + c16) * 1024 + (64 * j + 32 * (w & 1) + 8 * g4) * 2, 0, OAUX);
  }
  if (bid == 0 && tid == 0) put_flag(ws, WS_E, tag);
  STAMP(18);
  asm volatile(".Lfused_block_tail:");
  }
}

extern "C" void kernel_launch(void* const* d_in, const int* in_sizes, int n_in, void* d_out, int out_size, void* d_ws,
                              size_t ws_size, hipStream_t stream) {
  typedef const unsigned char* cp;
  fused_block_kernel<<<256, 512, 0, stream>>>((cp)d_in[0], (cp)d_in[1], (cp)d_in[2], (cp)d_in[3], (cp)d_in[4], (cp)d_in[6],
                                              (cp)d_in[7], (cp)d_in[8], (unsigned char*)d_out, (unsigned char*)d_ws);
}
```

```cpp
#include <hip/hip_runtime.h>
#include <hip/hip_bf16.h>

typedef unsigned long long u64;
typedef __attribute__((ext_vector_type(8))) __bf16 bf8;
typedef __attribute__((ext_vector_type(2))) __bf16 bf2;
typedef __attribute__((ext_vector_type(4))) short s4;
typedef __attribute__((ext_vector_type(2))) float f2;
typedef __attribute__((ext_vector_type(4))) float f4;
typedef __attribute__((ext_vector_type(16))) float f16v;
typedef __attribute__((ext_vector_type(2))) unsigned u2;
typedef __attribute__((ext_vector_type(4))) unsigned u4;
#define AS1 __attribute__((address_space(1)))
#define AS3 __attribute__((address_space(3)))
#define DEVI __device__ __forceinline__
#define RLX_AGENT __ATOMIC_RELAXED, __HIP_MEMORY_SCOPE_AGENT

constexpr unsigned WS_E = 0, WS_F1 = 4096, WS_KMAX = 20480;
constexpr unsigned WS_F2 = 0x30000, WS_F3 = 0x38000, WS_F4 = 0x40000;
constexpr unsigned WS_K = 65536, WS_V = 131072, WS_O = 1u << 20, WS_H = 3u << 20, WS_ACT = 5u << 20, WS_END = 16u << 20;
constexpr int L_XN = 0, L_WQ = 65536, L_WKV = 131072, L_QX = 131072;
constexpr int L_K = 0, L_V = 65536, L_T1 = 131072;
constexpr int L_WO = 0, L_RED1 = 65536;
constexpr int L_H = 0, L_W2A = 65536, L_ST = 159744, L_SF = 160256, L_CS = 160768;
constexpr int L_RED2 = 0;
constexpr int LDS_BYTES = 163840;
constexpr unsigned SPIN_CAP = 400000;
constexpr float LOG2E = 1.4426950408889634f;
constexpr float SAFE_BOUND = 50.0f;

#define EARLY 1
#define SR 8
#define AD 4
#define KVS 1
#define PSL 2
#define PSL1 4
#define STAMP(i) do {} while (0)

DEVI void bar() { asm volatile("s_waitcnt lgkmcnt(0)\n\ts_barrier" ::: "memory"); }
DEVI void drain() { asm volatile("s_waitcnt vmcnt(0)" ::: "memory"); }
DEVI unsigned pk(float a, float b) { f2 v = {a, b}; return __builtin_bit_cast(unsigned, __builtin_convertvector(v, bf2)); }
DEVI float lo16(unsigned v) { return __uint_as_float(v << 16); }
DEVI float hi16(unsigned v) { return __uint_as_float(v & 0xffff0000u); }
DEVI float dot2(unsigned a, unsigned b, float c) {
  return __builtin_amdgcn_fdot2_f32_bf16(__builtin_bit_cast(bf2, a), __builtin_bit_cast(bf2, b), c, false);
}
DEVI f16v mfma32(u4 a, u4 b, f16v c) {
  return __builtin_amdgcn_mfma_f32_32x32x16_bf16(__builtin_bit_cast(bf8, a), __builtin_bit_cast(bf8, b), c, 0, 0, 0);
}
DEVI f4 mfma16(u4 a, u4 b, f4 c) {
  return __builtin_amdgcn_mfma_f32_16x16x32_bf16(__builtin_bit_cast(bf8, a), __builtin_bit_cast(bf8, b), c, 0, 0, 0);
}
DEVI u4 lds16(const char* p) { return *(const u4*)p; }
DEVI u2 ldstr(const char* p) { return __builtin_bit_cast(u2, __builtin_amdgcn_ds_read_tr16_b64_v4i16((AS3 s4*)p)); }
DEVI u4 ldstr2(const char* p, int d) { u2 a = ldstr(p), b = ldstr(p + d); return u4{a[0], a[1], b[0], b[1]}; }
DEVI void dma16(const void* g, char* l) { __builtin_amdgcn_global_load_lds((const AS1 void*)g, (AS3 void*)l, 16, 0, 0); }
template <int C> DEVI float dpp(float v) { return __int_as_float(__builtin_amdgcn_update_dpp(0, __float_as_int(v), C, 0xf, 0xf, true)); }
DEVI float swap32_add(float v) { auto r = __builtin_amdgcn_permlane32_swap(__float_as_uint(v), __float_as_uint(v), false, false); return __uint_as_float(r[0]) + __uint_as_float(r[1]); }
DEVI float swap16_add(float v) { auto r = __builtin_amdgcn_permlane16_swap(__float_as_uint(v), __float_as_uint(v), false, false); return __uint_as_float(r[0]) + __uint_as_float(r[1]); }
DEVI float swap32_max(float v) { auto r = __builtin_amdgcn_permlane32_swap(__float_as_uint(v), __float_as_uint(v), false, false); return fmaxf(__uint_as_float(r[0]), __uint_as_float(r[1])); }
DEVI float swap16_max(float v) { auto r = __builtin_amdgcn_permlane16_swap(__float_as_uint(v), __float_as_uint(v), false, false); return fmaxf(__uint_as_float(r[0]), __uint_as_float(r[1])); }
DEVI float max16(float v) {
  v = fmaxf(v, dpp<0xB1>(v)); v = fmaxf(v, dpp<0x4E>(v)); v = fmaxf(v, dpp<0x141>(v)); return fmaxf(v, dpp<0x140>(v));
}
DEVI void put_flag(unsigned char* ws, unsigned off, u64 tag) { __hip_atomic_store((AS1 u64*)(ws + off), tag, RLX_AGENT); }
template <int SL> DEVI void wait_flags(unsigned char* ws, unsigned off, u64 tag) {
  for (unsigned spins = 0; spins < SPIN_CAP; ++spins) {
    if (__all(__hip_atomic_load((AS1 u64*)(ws + off), RLX_AGENT) == tag)) break;
    __builtin_amdgcn_s_sleep(SL);
  }
  __builtin_amdgcn_fence(__ATOMIC_ACQUIRE, "wavefront");
  asm volatile("" ::: "memory");
}
#define LD_SC1(so, vo) __builtin_amdgcn_raw_buffer_load_b128(rs, (vo), (so), 16)
#define DMA_SC1(so, vo, dst) __builtin_amdgcn_raw_ptr_buffer_load_lds(rs, (AS3 void*)(smem + (dst)), 16, (vo), (so), 0, 16)
#define ST_SC1(v, so, vo) __builtin_amdgcn_raw_buffer_store_b128((v), rs, (vo) + (so), 0, 16)

DEVI void pv_tile(const f16v& p, u4 v0, u4 v1, f16v& o) {
  u4 p0 = {pk(p[0], p[1]), pk(p[2], p[3]), pk(p[4], p[5]), pk(p[6], p[7])};
  o = mfma32(v0, p0, o);
  u4 p1 = {pk(p[8], p[9]), pk(p[10], p[11]), pk(p[12], p[13]), pk(p[14], p[15])};
  o = mfma32(v1, p1, o);
}
DEVI void tile_slow(const char* kp, const char* vp, bool vreal, unsigned vc, u4 q, float& m, f16v& o) {
  u4 v0 = lds16(vp), v1 = lds16(vp + 512);
#pragma unroll
  for (int e = 0; e < 4; ++e) { v0[e] = vreal ? v0[e] : vc; v1[e] = vreal ? v1[e] : vc; }
  f16v z = {0};
  f16v s = mfma32(lds16(kp), q, z);
  float t = s[0];
#pragma unroll
  for (int i = 1; i < 16; ++i) t = fmaxf(t, s[i]);
  float mn = fmaxf(m, swap32_max(t));
  float al = __builtin_amdgcn_exp2f(m - mn);
  m = mn;
  f16v p;
#pragma unroll
  for (int i = 0; i < 16; ++i) { p[i] = __builtin_amdgcn_exp2f(s[i] - mn); o[i] *= al; }
  pv_tile(p, v0, v1, o);
}

__global__ __launch_bounds__(512, 2) void fused_block_kernel(
    const unsigned char* __restrict__ x, const unsigned char* __restrict__ wq, const unsigned char* __restrict__ wk,
    const unsigned char* __restrict__ wv, const unsigned char* __restrict__ wo, const unsigned char* __restrict__ w1,
    const unsigned char* __restrict__ b1, const unsigned char* __restrict__ w2, unsigned char* __restrict__ out,
    unsigned char* ws) {
  __shared__ __attribute__((aligned(16))) char smem[LDS_BYTES];
  const int tid = threadIdx.x, lane = tid & 63;
  const int w = __builtin_amdgcn_readfirstlane(tid >> 6);
  const int bid = blockIdx.x;
  const int rb = (bid & 7) * 4 + ((bid >> 3) & 3), j = bid >> 5;
  const int R0 = rb * 64;
  const bool kvp = (j & 3) == (rb & 3);
  const int c16 = lane & 15, g4 = lane >> 4, r32 = lane & 31, h2 = lane >> 5;
  const __amdgpu_buffer_rsrc_t rs = __builtin_amdgcn_make_buffer_rsrc((void*)ws, 0, (int)WS_END, 0x00020000);
  STAMP(0);
  u2 e0;
  asm volatile("global_load_dwordx2 %0, %1, %2 sc1" : "=&v"(e0) : "v"(WS_E), "s"(ws) : "memory");
  const unsigned lds0 = (unsigned)(size_t)(AS3 char*)smem;
#define V_DMA(v, src, dst) asm volatile("s_mov_b32 m0, %2\n\ts_nop 0\n\tglobal_load_lds_dwordx4 %0, %1" :: "v"(v), "s"(src), "s"(lds0 + (dst)) : "memory", "m0")

  {
    const unsigned char* xr = x + (size_t)(R0 + 8 * w) * 1024;
    const unsigned xo = (lane >> 3) * 1024 + (lane & 7) * 16;
    u4 xv[8];
#pragma unroll
    for (int i = 0; i < 8; ++i) asm volatile("global_load_dwordx4 %0, %1, %2 offset:%3" : "=&v"(xv[i]) : "v"(xo), "s"(xr), "i"(i * 128));
    if (kvp) {
      const unsigned lo = lane * 16;
#pragma unroll
      for (int i = 0; i < 4; ++i) V_DMA(lo, ((i >> 1) ? wv : wk) + (w * 2 + (i & 1)) * 1024, L_WKV + (i >> 1) * 16384 + (w * 2 + (i & 1)) * 1024);
    }
    const int pc = lane & 7, f = ((lane >> 4) & 1) | ((w & 1) << 1);
    const unsigned qo = (lane >> 3) * 1024 + ((((pc >> 1) ^ f) << 1) | (pc & 1)) * 16;
#pragma unroll
    for (int i = 0; i < 8; ++i) V_DMA(qo, wq + (size_t)(i * 64 + w * 8) * 1024 + j * 128, L_WQ + (i * 8 + w) * 1024);
    asm volatile("s_waitcnt vmcnt(8)"
                 : "+v"(e0), "+v"(xv[0]), "+v"(xv[1]), "+v"(xv[2]), "+v"(xv[3]), "+v"(xv[4]), "+v"(xv[5]), "+v"(xv[6]), "+v"(xv[7]) :: "memory");
    float s1 = 0.f, s2 = 0.f;
#pragma unroll
    for (int i = 0; i < 8; ++i)
#pragma unroll
      for (int e = 0; e < 4; ++e) { s1 = dot2(xv[i][e], 0x3f803f80u, s1); s2 = dot2(xv[i][e], xv[i][e], s2); }
    s1 += dpp<0xB1>(s1); s2 += dpp<0xB1>(s2);
    s1 += dpp<0x4E>(s1); s2 += dpp<0x4E>(s2);
    s1 += dpp<0x141>(s1); s2 += dpp<0x141>(s2);
    const float mean = s1 * (1.f / 512.f);
    const float rstd = __builtin_amdgcn_rsqf(fmaxf(s2 * (1.f / 512.f) - mean * mean, 0.f) + 1e-5f);
    const float nm = -mean * rstd;
    const int rl = 8 * w + (lane >> 3), q7 = lane & 7;
    char* xw = smem + L_XN + (((rl >> 4) * 16 + (q7 >> 2)) * 64 + (q7 & 3) * 16 + ((rl & 15) ^ q7)) * 16;
#pragma unroll
    for (int i = 0; i < 8; ++i) {
      u4 y;
#pragma unroll
      for (int e = 0; e < 4; ++e) y[e] = pk(fmaf(lo16(xv[i][e]), rstd, nm), fmaf(hi16(xv[i][e]), rstd, nm));
      *(u4*)(xw + i * 2048) = y;
    }
  }
  const u64 tag = (((u64)(unsigned)__builtin_amdgcn_readfirstlane(e0[1]) << 32) | (unsigned)__builtin_amdgcn_readfirstlane(e0[0])) + 1;
  STAMP(1);
  bar();
  STAMP(2);
  const int xo0 = (g4 * 16 + (c16 ^ g4)) * 16, xo1 = (g4 * 16 + (c16 ^ (4 + g4))) * 16;
  if (kvp && w < 4) {
    const int kg = 2 * (j >> 2) + (w >> 1);
    const char* wp = smem + L_WKV + (w & 1) * 16384 + (8 * g4 + (c16 >> 2)) * 32 + (c16 & 3) * 8;
    const char* xp = smem + L_XN + kg * 16384;
    f4 a0 = {0}, a1 = {0};
    u4 wf[16], xf[16];
#pragma unroll
    for (int s = 0; s < 16; ++s) { wf[s] = ldstr2(wp + s * 1024, 128); xf[s] = lds16(xp + s * 1024 + ((s & 1) ? xo1 : xo0)); }
    __builtin_amdgcn_sched_barrier(0);
    if (w & 1) {
#pragma unroll
      for (int s = 0; s < 16; s += 2) { a0 = mfma16(xf[s], wf[s], a0); a1 = mfma16(xf[s + 1], wf[s + 1], a1); }
    } else {
#pragma unroll
      for (int s = 0; s < 16; s += 2) { a0 = mfma16(wf[s], xf[s], a0); a1 = mfma16(wf[s + 1], xf[s + 1], a1); }
    }
    a0 += a1;
    u2 kv = {pk(a0[0], a0[1]), pk(a0[2], a0[3])};
    const int key0 = R0 + 16 * kg;
    if (w & 1) {
      __builtin_amdgcn_raw_buffer_store_b64(kv, rs, WS_V + (key0 >> 4) * 512 + (g4 & 1) * 256 + c16 * 16 + (g4 >> 1) * 8, 0, 16);
      asm volatile("s_waitcnt vmcnt(1)" ::: "memory");
    } else {
      __builtin_amdgcn_raw_buffer_store_b64(kv, rs, WS_K + (key0 >> 5) * 1024 + ((g4 >> 1) * 32 + (key0 & 16) + c16) * 16 + (g4 & 1) * 8, 0, 16);
      float n2 = dot2(kv[0], kv[0], dot2(kv[1], kv[1], 0.f));
      n2 = max16(swap32_add(swap16_add(n2)));
      if (lane == 0) __builtin_amdgcn_raw_buffer_store_b32(__float_as_uint(n2), rs, WS_KMAX + (rb * 4 + kg) * 4, 0, 16);
      asm volatile("s_waitcnt vmcnt(2)" ::: "memory");
    }
  } else {
    drain();
  }
  bar();
  STAMP(3);
  const int rt = w & 1, ha = w >> 1;
  u4 qf;
  {
    const int f = ((c16 >> 3) & 1) | ((g4 & 1) << 1);
    const char* wp = smem + L_WQ + (8 * g4 + (c16 >> 2)) * 128 + ((ha ^ f) * 2) * 16 + (c16 & 3) * 8;
    const char* xp = smem + L_XN + rt * 32768;
    f4 a0 = {0}, a1 = {0};
    u4 wf[16], x0[16], x1[16];
#define Q_READ(s) do { wf[s] = ldstr2(wp + (s) * 4096, 512); x0[s] = lds16(xp + (s) * 1024 + (((s) & 1) ? xo1 : xo0)); \
                       x1[s] = lds16(xp + 16384 + (s) * 1024 + (((s) & 1) ? xo1 : xo0)); } while (0)
    Q_READ(0); Q_READ(1); Q_READ(2);
#pragma unroll
    for (int s = 0; s < 16; ++s) {
      __builtin_amdgcn_sched_barrier(0);
      if (s + 3 < 16) Q_READ(s + 3);
      __builtin_amdgcn_sched_barrier(0);
      a0 = mfma16(wf[s], x0[s], a0);
      a1 = mfma16(wf[s], x1[s], a1);
      if (s == KVS && kvp) {
        drain();
        bar();
        if (tid == 0) put_flag(ws, WS_F1 + (rb * 2 + (j >> 2)) * 128, tag);
      }
    }
    const float qs = 0.25f * LOG2E;
    auto r0 = __builtin_amdgcn_permlane16_swap(pk(a0[0] * qs, a0[1] * qs), pk(a1[0] * qs, a1[1] * qs), false, false);
    auto r1 = __builtin_amdgcn_permlane16_swap(pk(a0[2] * qs, a0[3] * qs), pk(a1[2] * qs, a1[3] * qs), false, false);
    qf = u4{r0[0], r1[0], r0[1], r1[1]};
    *(u4*)(smem + L_QX + w * 1024 + lane * 16) = qf;
  }
  STAMP(4);
  if (w == 0) wait_flags<PSL1>(ws, WS_F1 + lane * 128, tag);
  bar();
  STAMP(5);

  u2 w1f[16][4];
  const __amdgpu_buffer_rsrc_t rw1 = __builtin_amdgcn_make_buffer_rsrc((void*)w1, 0, 512 * 3584, 0x00020000);
  const int w1o = w < 7 ? (lane >> 2) * 3584 + (224 * j + 32 * w) * 2 + ((lane & 3) ^ ((((lane >> 3) ^ (lane >> 4)) & 1) << 1)) * 16 : 0x40000000;
#define W1_DMA(i) __builtin_amdgcn_raw_ptr_buffer_load_lds(rw1, (AS3 void*)(smem + L_T1 + w * 4096 + ((i) & 3) * 1024), 16, w1o, 16 * (i) * 3584, 0, 0)
  const unsigned t0 = (unsigned)(size_t)(AS3 char*)(smem + L_T1 + w * 4096 + (16 * (g4 >> 1) + 4 * (g4 & 1) + (c16 >> 2)) * 64 +
                                                    ((((c16 >> 3) ^ g4) & 1) * 2 + ((c16 >> 1) & 1)) * 16 + (c16 & 1) * 8);
  const unsigned t1 = t0 ^ 32;
#define W1_READ(s, n) asm volatile("s_waitcnt vmcnt(%8)\n\tds_read_b64_tr_b16 %0, %4 offset:%6\n\tds_read_b64_tr_b16 %1, %4 offset:%7\n\t" \
    "ds_read_b64_tr_b16 %2, %5 offset:%6\n\tds_read_b64_tr_b16 %3, %5 offset:%7" \
    : "=&v"(w1f[s][0]), "=&v"(w1f[s][1]), "=&v"(w1f[s][2]), "=&v"(w1f[s][3]) : "v"(t0), "v"(t1), "i"(((s) & 1) * 2048), "i"(((s) & 1) * 2048 + 512), "i"(n))
  f16v o;
  u2 xres0, xres1;
  {
    const int sw = (w >> 2) * 32768 + (w & 3) * 1024, so = sw + lane * 16;
#define KV_LOAD(i) do { DMA_SC1(WS_K + (i) * 4096, so, L_K + sw + (i) * 4096); DMA_SC1(WS_V + (i) * 4096, so, L_V + sw + (i) * 4096); } while (0)
    const u4 qb = lds16(smem + L_QX + (w ^ 1) * 1024 + lane * 16);
    asm volatile("" ::: "memory");
    KV_LOAD(0);
    const u2 km = __builtin_bit_cast(u2, __builtin_amdgcn_raw_buffer_load_b64(rs, WS_KMAX + lane * 8, 0, 16));
    const float qn = fmaxf(swap32_add(dot2(qf[0], qf[0], dot2(qf[1], qf[1], dot2(qf[2], qf[2], dot2(qf[3], qf[3], 0.f))))),
                           swap32_add(dot2(qb[0], qb[0], dot2(qb[1], qb[1], dot2(qb[2], qb[2], dot2(qb[3], qb[3], 0.f))))));
    float kn = fmaxf(__uint_as_float(km[0]), __uint_as_float(km[1]));
    kn = swap32_max(swap16_max(max16(kn)));
    const bool safe = __all(qn * kn <= SAFE_BOUND * SAFE_BOUND);
    drain();
    bar();
    STAMP(6);
    const bool vreal = r32 < 16;
    const unsigned vc = (r32 == 16 || r32 == 20) ? 0x3f803f80u : 0u;
    const unsigned ka = (unsigned)(size_t)(AS3 char*)(smem + L_K + rt * 32768 + lane * 16);
    const unsigned va = (unsigned)(size_t)(AS3 char*)(smem + L_V + rt * 32768 + h2 * 256 + c16 * 16);
    const u64 vm = 0x0000ffff0000ffffull;
    u4 kf, v0 = {vc, vc, vc, vc}, v1 = v0, p0, p1 = {0, 0, 0, 0};
#define K_READ(u) asm volatile("ds_read_b128 %0, %1 offset:%2" : "=v"(kf) : "v"(ka), "i"((u) * 1024))
#define V_READ(v, u, hf) asm volatile("s_mov_b64 exec, %3\n\tds_read_b128 %0, %1 offset:%2\n\ts_mov_b64 exec, -1" : "+v"(v) : "v"(va), "i"((u) * 1024 + (hf) * 512), "s"(vm))
#define LGKM(n, v) asm volatile("s_waitcnt lgkmcnt(" #n ")")
#define SB() __builtin_amdgcn_sched_barrier(0)
#define EXPS(s, a, b) _Pragma("unroll") for (int i = a; i < b; ++i) asm volatile("v_exp_f32 %0, %0" : "+v"(s[i]))
#define PKS(s, b) u4{pk(s[b], s[b + 1]), pk(s[b + 2], s[b + 3]), pk(s[b + 4], s[b + 5]), pk(s[b + 6], s[b + 7])}
    const f16v zero = {0};
    f16v oa = zero, ob = zero, sa, sb;
    unsigned pf0 = 0;
    const unsigned pfo = (w * 8 + (lane & 7)) * 1024;
#define PV(o, v, p) asm volatile("v_mfma_f32_32x32x16_bf16 %0, %1, %2, %0" : "+v"(o) : "v"(v), "v"(p))
#define QK(s, q) asm volatile("v_mfma_f32_32x32x16_bf16 %0, %1, %2, 0" : "=&v"(s) : "v"(kf), "v"(q))
    W1_DMA(0); W1_DMA(1);
    if (w >= 4) __builtin_amdgcn_s_setprio(1);
    K_READ(0); V_READ(v0, 0, 0);
    LGKM(1, kf);
    QK(sa, qf);
    SB();
    asm volatile("s_nop 7\n\ts_nop 7\n\ts_nop 7" : "+v"(sa), "+v"(oa), "+v"(ob), "+v"(p1), "+v"(v1));
#pragma unroll
    for (int u = 0; u < 32; ++u) {
      SB();
      if (u == 3) asm volatile("s_waitcnt vmcnt(11)" ::: "memory");
      if (u == 8) asm volatile("s_waitcnt vmcnt(7)" ::: "memory");
      if (u == 3 || u == 8) bar();
      if (u < 4) { KV_LOAD(2 * u + 1); if (u < 3) KV_LOAD(2 * u + 2); }
      if (u < 30) W1_DMA(u + 2);
      SB();
      EXPS(sa, 0, 2); SB();
      PV(ob, v1, p1); SB();
      V_READ(v1, u, 1);
      EXPS(sa, 2, 6); SB();
      QK(sb, qb); SB();
      if (u < 31) K_READ(u + 1); else K_READ(u);
      if (u & 1) W1_READ(u >> 1, u == 1 ? 10 : u == 3 ? 8 : u == 11 ? 5 : u == 31 ? 0 : (u >= 7 && u <= 23) ? 3 : 2);
      EXPS(sa, 6, 8); SB();
      p0 = PKS(sa, 0); SB();
      EXPS(sa, 8, 10);
      if (u & 1) LGKM(6, v0); else LGKM(2, v0);
      SB();
      PV(oa, v0, p0); SB();
      EXPS(sa, 10, 16); SB();
      p1 = PKS(sa, 8); SB();
      if ((u & 1) && u >= 5 && u <= 21) {
        const int ix = (u - 5) >> 1, cl = (bid >> 3) & 3;
        const unsigned char* pb = (ix < 7 ? w2 + (size_t)(cl * 448 + ix * 64) * 1024 : wo + (size_t)(cl * 128 + (ix - 7) * 64) * 1024) + j * 128;
        asm volatile("s_mov_b64 exec, 0xff\n\tglobal_load_dword %0, %1, %2\n\ts_mov_b64 exec, -1" : "+v"(pf0) : "v"(pfo), "s"(pb));
      }
      if (u == 9) {
        const unsigned char* xr = x + (size_t)(R0 + 32 * rt + r32) * 1024 + (64 * j + 16 * ha + 4 * h2) * 2;
        xres0 = *(const u2*)xr; xres1 = *(const u2*)(xr + 16);
      }
      if (u == 31) asm volatile("" :: "v"(pf0));

      SB();
      EXPS(sb, 0, 2);
      if (u & 1) LGKM(5, v1); else LGKM(1, v1);
      SB();
      PV(oa, v1, p1); SB();
      EXPS(sb, 2, 6);
      LGKM(0, kf);
      if (u & 1) asm volatile("" : "+v"(w1f[u >> 1][0]), "+v"(w1f[u >> 1][1]), "+v"(w1f[u >> 1][2]), "+v"(w1f[u >> 1][3]));
      SB();
      QK(sa, qf); SB();
      EXPS(sb, 6, 8); SB();
      p0 = PKS(sb, 0); SB();
      EXPS(sb, 8, 10); SB();
      PV(ob, v0, p0); SB();
      if (u < 31) V_READ(v0, u + 1, 0);
      EXPS(sb, 10, 16); SB();
      p1 = PKS(sb, 8);
    }
    __builtin_amdgcn_s_setprio(0);
    asm volatile("s_nop 1");
    PV(ob, v1, p1);
    asm volatile("s_nop 7\n\ts_nop 7\n\ts_nop 7" : "+v"(oa), "+v"(ob));
    SB();
    char* xo = smem + L_T1 + w * 4096 + lane * 16;
    *(f4*)xo = f4{ob[0], ob[1], ob[2], ob[3]};
    *(f4*)(xo + 1024) = f4{ob[4], ob[5], ob[6], ob[7]};
    *(float*)(smem + L_T1 + w * 4096 + 2048 + lane * 4) = ob[8];
    if (!safe) {
      float m = -3.0e38f;
      oa = zero;
      const char* kp = smem + L_K + lane * 16;
      const char* vp = smem + L_V + h2 * 256 + c16 * 16;
#pragma unroll 1
      for (int t = 0; t < 64; ++t) tile_slow(kp + t * 1024, vp + t * 1024, vreal, vc, qf, m, oa);
    }
    bar();
    o = oa;
    if (safe) {
      const char* xi = smem + L_T1 + (w ^ 1) * 4096 + lane * 16;
      const f4 a = *(const f4*)xi, b = *(const f4*)(xi + 1024);
#pragma unroll
      for (int e = 0; e < 4; ++e) { o[e] += a[e]; o[4 + e] += b[e]; }
      o[8] += *(const float*)(smem + L_T1 + (w ^ 1) * 4096 + 2048 + lane * 4);
    }
  }
  STAMP(7);
  {
  int tid = threadIdx.x;
  asm volatile("" : "+v"(tid));
  const int lane = tid & 63, c16 = lane & 15, g4 = lane >> 4, h2 = lane >> 5;
  {
    const float il = __builtin_amdgcn_rcpf(o[8]);
    u4 of = {pk(o[0] * il, o[1] * il), pk(o[2] * il, o[3] * il), pk(o[4] * il, o[5] * il), pk(o[6] * il, o[7] * il)};
    ST_SC1(of, WS_O + ((rb * 2 + rt) * 32 + 4 * j + ha) * 1024, lane * 16);
  }
  asm volatile("" ::: "memory");
  const unsigned w2v = (lane >> 3) * 1024 + ((lane & 7) ^ (((lane >> 4) & 1) << 2)) * 16;
#define W_DMA(src, dst) V_DMA(w2v, src, dst)
#pragma unroll
  for (int i = 0; i < 8; ++i) W_DMA(wo + (size_t)(i * 64 + w * 8) * 1024 + j * 128, L_WO + (i * 8 + w) * 1024);
  asm volatile("s_waitcnt vmcnt(8)" ::: "memory");
  bar();
  if (tid == 0) put_flag(ws, WS_F2 + (rb * 8 + j) * 128, tag);
  STAMP(8);

  u4 a1f[16][2];
  f4 cs0 = {0}, cs1 = {0};
  const u4 ones = {0x3f803f80u, 0x3f803f80u, 0x3f803f80u, 0x3f803f80u};
#pragma unroll
  for (int s = 0; s < 16; ++s) {
    a1f[s][0] = u4{w1f[s][0][0], w1f[s][0][1], w1f[s][1][0], w1f[s][1][1]};
    a1f[s][1] = u4{w1f[s][2][0], w1f[s][2][1], w1f[s][3][0], w1f[s][3][1]};
    cs0 = mfma16(a1f[s][0], ones, cs0); cs1 = mfma16(a1f[s][1], ones, cs1);
  }
  const unsigned char* bp = b1 + (224 * j + 32 * (w < 7 ? w : 6) + 4 * g4) * 2;
  const u2 ba = *(const u2*)bp, bb = *(const u2*)(bp + 32);
  if (tid == 0) *(volatile AS3 unsigned*)(AS3 char*)(smem + L_SF) = 0;
  if (c16 == 0) {
    char* cp = smem + L_CS + (w * 4 + g4) * 64;
    *(f4*)cp = cs0; *(f4*)(cp + 16) = cs1;
    *(f4*)(cp + 32) = f4{lo16(ba[0]), hi16(ba[0]), lo16(ba[1]), hi16(ba[1])};
    *(f4*)(cp + 48) = f4{lo16(bb[0]), hi16(bb[0]), lo16(bb[1]), hi16(bb[1])};
  }
  const unsigned char* w2p = w2 + (size_t)(224 * w) * 1024 + j * 128;
#define W2_DMA(s, dst) _Pragma("unroll") for (int q = 0; q < 4; ++q) W_DMA(w2p + (size_t)(32 * (s) + 8 * q) * 1024, (dst) + q * 1024)
  const int kq = ha;
  wait_flags<PSL>(ws, WS_F2 + (rb * 8 + 2 * kq + (lane & 1)) * 128, tag);
  STAMP(9);
  float hv[8];
  {
    u4 ob[8];
#pragma unroll
    for (int i = 0; i < 8; ++i) { ob[i] = LD_SC1(WS_O + ((rb * 2 + rt) * 32 + 8 * kq + i) * 1024, lane * 16); SB(); }
    asm volatile("s_waitcnt vmcnt(8)" ::: "memory");
    bar();
    const int q = c16 >> 2, u = g4 & 1;
    const char* wp = smem + L_WO + (kq * 128 + 4 * h2 + q) * 128 + (c16 & 3) * 8;
    const int po0 = (u ^ ((q >> 1) << 1)) * 32, po1 = ((2 + u) ^ ((q >> 1) << 1)) * 32;
    f16v acc[2] = {{0}, {0}};
#pragma unroll
    for (int i = 0; i < 8; ++i) {
      acc[0] = mfma32(ldstr2(wp + i * 2048 + po0, 1024), ob[i], acc[0]);
      acc[1] = mfma32(ldstr2(wp + i * 2048 + po1, 1024), ob[i], acc[1]);
    }
    STAMP(10);
    if (w < 7) { W2_DMA(4, L_T1 + w * 4096); }
#pragma unroll
    for (int d = 0; d < 4; ++d) {
      if (d != kq) {
        char* rp = smem + L_RED1 + ((rt + 2 * d) * 4 + kq) * 2048 + lane * 16;
        const f16v& a = acc[d >> 1];
        const int b = 8 * (d & 1);
        *(f4*)rp = f4{a[b], a[b + 1], a[b + 2], a[b + 3]};
        *(f4*)(rp + 1024) = f4{a[b + 4], a[b + 5], a[b + 6], a[b + 7]};
      } else {
#pragma unroll
        for (int e = 0; e < 8; ++e) hv[e] = acc[d >> 1][8 * (d & 1) + e];
      }
    }
    bar();
#pragma unroll
    for (int d = 1; d < 4; ++d) {
      const char* rp = smem + L_RED1 + (w * 4 + ((kq + d) & 3)) * 2048 + lane * 16;
      f4 a = *(const f4*)rp, b = *(const f4*)(rp + 1024);
#pragma unroll
      for (int e = 0; e < 4; ++e) { hv[e] += a[e]; hv[4 + e] += b[e]; }
    }
    u4 hf = {pk(hv[0] + lo16(xres0[0]), hv[1] + hi16(xres0[0])), pk(hv[2] + lo16(xres0[1]), hv[3] + hi16(xres0[1])),
             pk(hv[4] + lo16(xres1[0]), hv[5] + hi16(xres1[0])), pk(hv[6] + lo16(xres1[1]), hv[7] + hi16(xres1[1]))};
    ST_SC1(hf, WS_H + ((rb * 2 + rt) * 32 + 4 * j + kq) * 1024, lane * 16);
  }
  drain();
  bar();
  if (tid == 0) put_flag(ws, WS_F3 + (rb * 8 + j) * 128, tag);
  STAMP(11);

  if (EARLY > 0) { W2_DMA(0, L_W2A + w * 8192); }
  if (EARLY > 1) { W2_DMA(1, L_W2A + w * 8192 + 4096); }
  wait_flags<PSL>(ws, WS_F3 + (rb * 8 + w) * 128, tag);
  STAMP(12);
  {
#pragma unroll
    for (int i = 0; i < 8; ++i)
      DMA_SC1(WS_H + ((rb * 2 + (i >> 2)) * 32 + 4 * w + (i & 3)) * 1024, lane * 16, L_H + ((i >> 2) * 32 + 4 * w + (i & 3)) * 1024);
    drain();
  }
  bar();
  STAMP(13);
  u4 af[7][4];
  const int ro = (4 * g4 + (c16 >> 2)) * 128, co0 = ((c16 & 3) ^ (((c16 >> 3) & 1) << 2)) * 16, co1 = co0 ^ 64;
#define W2_FRAG(base, t) ldstr2(smem + (base) + ro + (((t) >> 1) ? co1 : co0) + ((t) & 1) * 8, 2048)
#define W2_REGS() drain(); _Pragma("unroll") for (int t = 0; t < 4; ++t) { af[0][t] = W2_FRAG(L_W2A + w * 8192, t); af[1][t] = W2_FRAG(L_W2A + w * 8192 + 4096, t); }
#define W2_NEXT() do { asm volatile("s_waitcnt lgkmcnt(0)" ::: "memory"); W2_DMA(5, L_W2A + w * 8192); W2_DMA(6, L_W2A + w * 8192 + 4096); } while (0)
  u2 hr0, hr1;
  {
    const int mt = w >> 1;
    const char* hp = smem + L_H + ((mt >> 1) * 32 + 4 * j + 2 * (w & 1) + (g4 >> 1)) * 1024 + (16 * (mt & 1) + c16) * 16 + (g4 & 1) * 8;
    hr0 = *(const u2*)hp; hr1 = *(const u2*)(hp + 512);
  }
  {
    const char* hp = smem + L_H + (g4 >> 1) * 1024 + ((g4 & 1) * 32 + c16) * 16;
#define H_READ(s, mt) lds16(hp + ((mt) >> 1) * 32768 + (s) * 2048 + ((mt) & 1) * 256)
    if (w == 7) {
      f4 s1[4] = {}, s2[4] = {};
      u4 hb[2][4];
      __builtin_amdgcn_s_setprio(2);
#pragma unroll
      for (int mt = 0; mt < 4; ++mt) hb[0][mt] = H_READ(0, mt);
#pragma unroll
      for (int s = 0; s < 16; ++s) {
        if (s < 15) {
#pragma unroll
          for (int mt = 0; mt < 4; ++mt) hb[(s + 1) & 1][mt] = H_READ(s + 1, mt);
        }
        if (s == 0 && EARLY < 1) { W2_DMA(0, L_W2A + w * 8192); }
        if (s == 2 && EARLY < 2) { W2_DMA(1, L_W2A + w * 8192 + 4096); }
        SB();
#pragma unroll
        for (int mt = 0; mt < 4; ++mt) { s1[mt] = mfma16(ones, hb[s & 1][mt], s1[mt]); s2[mt] = mfma16(hb[s & 1][mt], hb[s & 1][mt], s2[mt]); }
        SB();
      }
#pragma unroll
      for (int mt = 0; mt < 4; ++mt) {
        const float mean = s1[mt][0] * (1.f / 512.f);
        const float sq = (c16 & 2) ? ((c16 & 1) ? s2[mt][3] : s2[mt][2]) : ((c16 & 1) ? s2[mt][1] : s2[mt][0]);
        const float rstd = __builtin_amdgcn_rsqf(fmaxf(sq * (1.f / 512.f) - mean * mean, 0.f) + 1e-5f);
        if (g4 == (c16 >> 2)) *(f2*)(smem + L_ST + (16 * mt + c16) * 8) = f2{mean, rstd};
      }
      asm volatile("s_waitcnt lgkmcnt(0)" ::: "memory");
      if (lane == 0) *(volatile AS3 unsigned*)(AS3 char*)(smem + L_SF) = 1;
      W2_REGS();
      W2_NEXT();
      if (bid == 0) {
#pragma unroll 1
        for (int g = 0; g < 4; ++g) wait_flags<PSL>(ws, WS_F2 + (g * 64 + lane) * 128, tag);
      }
    } else {
      f4 acc[2][4] = {};
      u4 hb[2][4];
#pragma unroll
      for (int mt = 0; mt < 4; ++mt) hb[0][mt] = H_READ(0, mt);
#pragma unroll
      for (int s = 0; s < 16; ++s) {
        if (s < 15) {
#pragma unroll
          for (int mt = 0; mt < 4; ++mt) hb[(s + 1) & 1][mt] = H_READ(s + 1, mt);
        }
        if (s == 0 && EARLY < 1) { W2_DMA(0, L_W2A + w * 8192); }
        if (s == 2 && EARLY < 2) { W2_DMA(1, L_W2A + w * 8192 + 4096); }
        if (s == SR) { W2_REGS(); }
        if (s == SR + 2) { asm volatile("s_waitcnt lgkmcnt(0)" ::: "memory"); W2_DMA(5, L_W2A + w * 8192); }
        if (s == SR + 4) { W2_DMA(6, L_W2A + w * 8192 + 4096); }
        SB();
#pragma unroll
        for (int mt = 0; mt < 4; ++mt) {
          acc[0][mt] = mfma16(a1f[s][0], hb[s & 1][mt], acc[0][mt]);
          acc[1][mt] = mfma16(a1f[s][1], hb[s & 1][mt], acc[1][mt]);
        }
        SB();
      }
      STAMP(14);
      while (*(volatile AS3 unsigned*)(AS3 char*)(smem + L_SF) == 0) __builtin_amdgcn_s_sleep(1);
      asm volatile("" ::: "memory");
      const char* cp = smem + L_CS + (w * 4 + g4) * 64;
      const f4 c0 = *(const f4*)cp, c1 = *(const f4*)(cp + 16), bb0 = *(const f4*)(cp + 32), bb1 = *(const f4*)(cp + 48);
      f2 stv[4];
#pragma unroll
      for (int mt = 0; mt < 4; ++mt) stv[mt] = *(const f2*)(smem + L_ST + (16 * mt + c16) * 8);
#pragma unroll
      for (int mt = 0; mt < 4; ++mt) {
        const f2 st = stv[mt];
        float y[8];
#pragma unroll
        for (int e = 0; e < 8; ++e) {
          const float cs = (e >> 2) ? c1[e & 3] : c0[e & 3];
          const float bb = (e >> 2) ? bb1[e & 3] : bb0[e & 3];
          const float v = fmaf(st[1], fmaf(-st[0], cs, acc[e >> 2][mt][e & 3]), bb);
          const float ex = __builtin_amdgcn_exp2f(v * LOG2E);
          const float r = __builtin_amdgcn_rcpf(fmaf(ex, ex + 2.f, 2.f));
          y[e] = fmaf(-2.f, v * r, v);
        }
        ST_SC1((u4{pk(y[0], y[1]), pk(y[2], y[3]), pk(y[4], y[5]), pk(y[6], y[7])}), WS_ACT + ((rb * 4 + mt) * 56 + 7 * j + w) * 1024, lane * 16);
      }
    }
  }
  drain();
  bar();
  __builtin_amdgcn_s_setprio(0);
  if (tid == 0) put_flag(ws, WS_F4 + (rb * 8 + j) * 128, tag);
  STAMP(15);

  {
    W2_DMA(2, L_H + w * 8192); W2_DMA(3, L_H + w * 8192 + 4096);
    if (w == 7) { W2_DMA(4, L_T1 + w * 4096); }
    wait_flags<PSL>(ws, WS_F4 + (rb * 8 + w) * 128, tag);
    STAMP(16);
    f4 acc[4][4] = {};
    u4 ab[7][4];
#define ACT_LOAD(s) _Pragma("unroll") for (int mt = 0; mt < 4; ++mt) ab[s][mt] = LD_SC1(WS_ACT + ((rb * 4 + mt) * 56 + 7 * w + (s)) * 1024, lane * 16)
    drain();
    asm volatile("s_waitcnt lgkmcnt(0)" : "+v"(af[0][0]), "+v"(af[0][1]), "+v"(af[0][2]), "+v"(af[0][3]), "+v"(af[1][0]), "+v"(af[1][1]), "+v"(af[1][2]), "+v"(af[1][3]));
    SB(); ACT_LOAD(0); SB(); ACT_LOAD(1); SB(); ACT_LOAD(2); SB();
    if (AD > 3) { ACT_LOAD(3); }
#pragma unroll
    for (int s = 0; s < 7; ++s) {
      SB();
      if (s >= 1 && s < 6) {
        const int base = (s == 1 ? L_H : s == 2 ? L_H + 4096 : s == 4 ? L_W2A : L_W2A + 4096) + w * 8192;
#pragma unroll
        for (int t = 0; t < 4; ++t) af[s + 1][t] = W2_FRAG(s == 3 ? L_T1 + w * 4096 : base, t);
      }
      SB();
#pragma unroll
      for (int mt = 0; mt < 4; ++mt)
#pragma unroll
        for (int t = 0; t < 4; ++t) acc[t][mt] = mfma16(af[s][t], ab[s][mt], acc[t][mt]);
      SB();
      if (s + AD < 7) { ACT_LOAD(s + AD); }
    }
    STAMP(17);
    float y[8];
#pragma unroll
    for (int d = 0; d < 8; ++d) {
      const int mt = d >> 1, b = 2 * (d & 1);
      if (d != w) {
        const f4 a0 = acc[b][mt], a1 = acc[b + 1][mt];
        *(u4*)(smem + L_RED2 + (w * 8 + d) * 1024 + lane * 16) = u4{pk(a0[0], a0[1]), pk(a0[2], a0[3]), pk(a1[0], a1[1]), pk(a1[2], a1[3])};
      } else {
#pragma unroll
        for (int e = 0; e < 8; ++e) y[e] = acc[b + (e >> 2)][mt][e & 3];
      }
    }
    bar();
#pragma unroll
    for (int d = 1; d < 8; ++d) {
      const u4 a = lds16(smem + L_RED2 + (((w + d) & 7) * 8 + w) * 1024 + lane * 16);
#pragma unroll
      for (int e = 0; e < 4; ++e) { y[2 * e] += lo16(a[e]); y[2 * e + 1] += hi16(a[e]); }
    }
    u4 r = {pk(y[0] + lo16(hr0[0]), y[1] + hi16(hr0[0])), pk(y[2] + lo16(hr0[1]), y[3] + hi16(hr0[1])),
            pk(y[4] + lo16(hr1[0]), y[5] + hi16(hr1[0])), pk(y[6] + lo16(hr1[1]), y[7] + hi16(hr1[1]))};
    const __amdgpu_buffer_rsrc_t ro_ = __builtin_amdgcn_make_buffer_rsrc((void*)out, 0, 2048 * 1024, 0x00020000);
    __builtin_amdgcn_raw_buffer_store_b128(r, ro_, (R0 + 16 * (w >> 1) + c16) * 1024 + (64 * j + 32 * (w & 1) + 8 * g4) * 2, 0, 0);
  }
  if (bid == 0 && tid == 0) put_flag(ws, WS_E, tag);
  STAMP(18);
  }
}

extern "C" void kernel_launch(void* const* d_in, const int* in_sizes, int n_in, void* d_out, int out_size, void* d_ws,
                              size_t ws_size, hipStream_t stream) {
  typedef const unsigned char* cp;
  fused_block_kernel<<<256, 512, 0, stream>>>((cp)d_in[0], (cp)d_in[1], (cp)d_in[2], (cp)d_in[3], (cp)d_in[4], (cp)d_in[6],
                                              (cp)d_in[7], (cp)d_in[8], (unsigned char*)d_out, (unsigned char*)d_ws);
}
```

```cpp
#include <hip/hip_runtime.h>
#include <hip/hip_bf16.h>

typedef unsigned long long u64;
typedef __attribute__((ext_vector_type(8))) __bf16 bf8;
typedef __attribute__((ext_vector_type(2))) __bf16 bf2;
typedef __attribute__((ext_vector_type(4))) short s4;
typedef __attribute__((ext_vector_type(2))) float f2;
typedef __attribute__((ext_vector_type(4))) float f4;
typedef __attribute__((ext_vector_type(16))) float f16v;
typedef __attribute__((ext_vector_type(2))) unsigned u2;
typedef __attribute__((ext_vector_type(4))) unsigned u4;
#define AS1 __attribute__((address_space(1)))
#define AS3 __attribute__((address_space(3)))
#define DEVI __device__ __forceinline__
#define RLX_AGENT __ATOMIC_RELAXED, __HIP_MEMORY_SCOPE_AGENT

constexpr unsigned WS_E = 0, WS_F1 = 4096, WS_KMAX = 20480;
constexpr unsigned WS_F2 = 0x30000, WS_F3 = 0x38000, WS_F4 = 0x40000;
constexpr unsigned WS_K = 65536, WS_V = 131072, WS_O = 1u << 20, WS_H = 3u << 20, WS_ACT = 5u << 20, WS_END = 16u << 20;
constexpr int L_XN = 0, L_WQ = 65536, L_WKV = 131072, L_QX = 131072;
constexpr int L_K = 0, L_V = 65536, L_T1 = 131072;
constexpr int L_WO = 0, L_RED1 = 65536;
constexpr int L_H = 0, L_W2A = 65536, L_ST = 159744, L_SF = 160256, L_CS = 160768;
constexpr int L_RED2 = 0;
constexpr int LDS_BYTES = 163840;
constexpr unsigned SPIN_CAP = 400000;
constexpr float LOG2E = 1.4426950408889634f;
constexpr float SAFE_BOUND = 50.0f;

#define EARLY 1
#define SR 8
#define AD 4
#define PFS 9
#define D4 1
#define KVS 1
#define PSL 2
#define PSL1 4
#define STAMP(i) do {} while (0)

DEVI void bar() { asm volatile("s_waitcnt lgkmcnt(0)\n\ts_barrier" ::: "memory"); }
DEVI void drain() { asm volatile("s_waitcnt vmcnt(0)" ::: "memory"); }
DEVI unsigned pk(float a, float b) { f2 v = {a, b}; return __builtin_bit_cast(unsigned, __builtin_convertvector(v, bf2)); }
DEVI float lo16(unsigned v) { return __uint_as_float(v << 16); }
DEVI float hi16(unsigned v) { return __uint_as_float(v & 0xffff0000u); }
DEVI float dot2(unsigned a, unsigned b, float c) {
  return __builtin_amdgcn_fdot2_f32_bf16(__builtin_bit_cast(bf2, a), __builtin_bit_cast(bf2, b), c, false);
}
DEVI f16v mfma32(u4 a, u4 b, f16v c) {
  return __builtin_amdgcn_mfma_f32_32x32x16_bf16(__builtin_bit_cast(bf8, a), __builtin_bit_cast(bf8, b), c, 0, 0, 0);
}
DEVI f4 mfma16(u4 a, u4 b, f4 c) {
  return __builtin_amdgcn_mfma_f32_16x16x32_bf16(__builtin_bit_cast(bf8, a), __builtin_bit_cast(bf8, b), c, 0, 0, 0);
}
DEVI u4 lds16(const char* p) { return *(const u4*)p; }
DEVI u2 ldstr(const char* p) { return __builtin_bit_cast(u2, __builtin_amdgcn_ds_read_tr16_b64_v4i16((AS3 s4*)p)); }
DEVI u4 ldstr2(const char* p, int d) { u2 a = ldstr(p), b = ldstr(p + d); return u4{a[0], a[1], b[0], b[1]}; }
template <int C> DEVI float dpp(float v) { return __int_as_float(__builtin_amdgcn_update_dpp(0, __float_as_int(v), C, 0xf, 0xf, true)); }
DEVI float swap32_add(float v) { auto r = __builtin_amdgcn_permlane32_swap(__float_as_uint(v), __float_as_uint(v), false, false); return __uint_as_float(r[0]) + __uint_as_float(r[1]); }
DEVI float swap16_add(float v) { auto r = __builtin_amdgcn_permlane16_swap(__float_as_uint(v), __float_as_uint(v), false, false); return __uint_as_float(r[0]) + __uint_as_float(r[1]); }
DEVI float swap32_max(float v) { auto r = __builtin_amdgcn_permlane32_swap(__float_as_uint(v), __float_as_uint(v), false, false); return fmaxf(__uint_as_float(r[0]), __uint_as_float(r[1])); }
DEVI float swap16_max(float v) { auto r = __builtin_amdgcn_permlane16_swap(__float_as_uint(v), __float_as_uint(v), false, false); return fmaxf(__uint_as_float(r[0]), __uint_as_float(r[1])); }
DEVI float max16(float v) {
  v = fmaxf(v, dpp<0xB1>(v)); v = fmaxf(v, dpp<0x4E>(v)); v = fmaxf(v, dpp<0x141>(v)); return fmaxf(v, dpp<0x140>(v));
}
DEVI void put_flag(unsigned char* ws, unsigned off, u64 tag) { __hip_atomic_store((AS1 u64*)(ws + off), tag, RLX_AGENT); }
template <int SL> DEVI void wait_flags(unsigned char* ws, unsigned off, u64 tag) {
  for (unsigned spins = 0; spins < SPIN_CAP; ++spins) {
    if (__all(__hip_atomic_load((AS1 u64*)(ws + off), RLX_AGENT) == tag)) break;
    __builtin_amdgcn_s_sleep(SL);
  }
  __builtin_amdgcn_fence(__ATOMIC_ACQUIRE, "wavefront");
  asm volatile("" ::: "memory");
}
#define LD_SC1(so, vo) __builtin_amdgcn_raw_buffer_load_b128(rs, (vo), (so), 16)
#define DMA_SC1(so, vo, dst) __builtin_amdgcn_raw_ptr_buffer_load_lds(rs, (AS3 void*)(smem + (dst)), 16, (vo), (so), 0, 16)
#define ST_SC1(v, so, vo) __builtin_amdgcn_raw_buffer_store_b128((v), rs, (vo) + (so), 0, 16)

DEVI void pv_tile(const f16v& p, u4 v0, u4 v1, f16v& o) {
  u4 p0 = {pk(p[0], p[1]), pk(p[2], p[3]), pk(p[4], p[5]), pk(p[6], p[7])};
  o = mfma32(v0, p0, o);
  u4 p1 = {pk(p[8], p[9]), pk(p[10], p[11]), pk(p[12], p[13]), pk(p[14], p[15])};
  o = mfma32(v1, p1, o);
}
DEVI void tile_slow(const char* kp, const char* vp, bool vreal, unsigned vc, u4 q, float& m, f16v& o) {
  u4 v0 = lds16(vp), v1 = lds16(vp + 512);
#pragma unroll
  for (int e = 0; e < 4; ++e) { v0[e] = vreal ? v0[e] : vc; v1[e] = vreal ? v1[e] : vc; }
  f16v z = {0};
  f16v s = mfma32(lds16(kp), q, z);
  float t = s[0];
#pragma unroll
  for (int i = 1; i < 16; ++i) t = fmaxf(t, s[i]);
  float mn = fmaxf(m, swap32_max(t));
  float al = __builtin_amdgcn_exp2f(m - mn);
  m = mn;
  f16v p;
#pragma unroll
  for (int i = 0; i < 16; ++i) { p[i] = __builtin_amdgcn_exp2f(s[i] - mn); o[i] *= al; }
  pv_tile(p, v0, v1, o);
}

__global__ __launch_bounds__(512, 2) void fused_block_kernel(
    const unsigned char* __restrict__ x, const unsigned char* __restrict__ wq, const unsigned char* __restrict__ wk,
    const unsigned char* __restrict__ wv, const unsigned char* __restrict__ wo, const unsigned char* __restrict__ w1,
    const unsigned char* __restrict__ b1, const unsigned char* __restrict__ w2, unsigned char* __restrict__ out,
    unsigned char* ws) {
  __shared__ __attribute__((aligned(16))) char smem[LDS_BYTES];
  const int tid = threadIdx.x, lane = tid & 63;
  const int w = __builtin_amdgcn_readfirstlane(tid >> 6);
  const int bid = blockIdx.x;
  const int rb = (bid & 7) * 4 + ((bid >> 3) & 3), j = bid >> 5;
  const int R0 = rb * 64;
  const bool kvp = (j & 3) == (rb & 3);
  const int c16 = lane & 15, g4 = lane >> 4, r32 = lane & 31, h2 = lane >> 5;
  const __amdgpu_buffer_rsrc_t rs = __builtin_amdgcn_make_buffer_rsrc((void*)ws, 0, (int)WS_END, 0x00020000);
  STAMP(0);
  u2 e0;
  asm volatile("global_load_dwordx2 %0, %1, %2 sc1" : "=&v"(e0) : "v"(WS_E), "s"(ws) : "memory");
  const unsigned lds0 = (unsigned)(size_t)(AS3 char*)smem;
#define V_DMA(v, src, dst) asm volatile("s_mov_b32 m0, %2\n\ts_nop 0\n\tglobal_load_lds_dwordx4 %0, %1" :: "v"(v), "s"(src), "s"(lds0 + (dst)) : "memory", "m0")

  {
    const unsigned char* xr = x + (size_t)(R0 + 8 * w) * 1024;
    const unsigned xo = (lane >> 3) * 1024 + (lane & 7) * 16;
    u4 xv[8];
#pragma unroll
    for (int i = 0; i < 8; ++i) asm volatile("global_load_dwordx4 %0, %1, %2 offset:%3" : "=&v"(xv[i]) : "v"(xo), "s"(xr), "i"(i * 128));
    if (kvp) {
      const unsigned lo = lane * 16;
#pragma unroll
      for (int i = 0; i < 4; ++i) V_DMA(lo, ((i >> 1) ? wv : wk) + (w * 2 + (i & 1)) * 1024, L_WKV + (i >> 1) * 16384 + (w * 2 + (i & 1)) * 1024);
    }
    const int pc = lane & 7, f = ((lane >> 4) & 1) | ((w & 1) << 1);
    const unsigned qo = (lane >> 3) * 1024 + ((((pc >> 1) ^ f) << 1) | (pc & 1)) * 16;
#pragma unroll
    for (int i = 0; i < 8; ++i) V_DMA(qo, wq + (size_t)(i * 64 + w * 8) * 1024 + j * 128, L_WQ + (i * 8 + w) * 1024);
    asm volatile("s_waitcnt vmcnt(8)"
                 : "+v"(e0), "+v"(xv[0]), "+v"(xv[1]), "+v"(xv[2]), "+v"(xv[3]), "+v"(xv[4]), "+v"(xv[5]), "+v"(xv[6]), "+v"(xv[7]) :: "memory");
    float s1 = 0.f, s2 = 0.f;
#pragma unroll
    for (int i = 0; i < 8; ++i)
#pragma unroll
      for (int e = 0; e < 4; ++e) { s1 = dot2(xv[i][e], 0x3f803f80u, s1); s2 = dot2(xv[i][e], xv[i][e], s2); }
    s1 += dpp<0xB1>(s1); s2 += dpp<0xB1>(s2);
    s1 += dpp<0x4E>(s1); s2 += dpp<0x4E>(s2);
    s1 += dpp<0x141>(s1); s2 += dpp<0x141>(s2);
    const float mean = s1 * (1.f / 512.f);
    const float rstd = __builtin_amdgcn_rsqf(fmaxf(s2 * (1.f / 512.f) - mean * mean, 0.f) + 1e-5f);
    const float nm = -mean * rstd;
    const int rl = 8 * w + (lane >> 3), q7 = lane & 7;
    char* xw = smem + L_XN + (((rl >> 4) * 16 + (q7 >> 2)) * 64 + (q7 & 3) * 16 + ((rl & 15) ^ q7)) * 16;
#pragma unroll
    for (int i = 0; i < 8; ++i) {
      u4 y;
#pragma unroll
      for (int e = 0; e < 4; ++e) y[e] = pk(fmaf(lo16(xv[i][e]), rstd, nm), fmaf(hi16(xv[i][e]), rstd, nm));
      *(u4*)(xw + i * 2048) = y;
    }
  }
  const u64 tag = (((u64)(unsigned)__builtin_amdgcn_readfirstlane(e0[1]) << 32) | (unsigned)__builtin_amdgcn_readfirstlane(e0[0])) + 1;
  STAMP(1);
  bar();
  STAMP(2);
  const int xo0 = (g4 * 16 + (c16 ^ g4)) * 16, xo1 = (g4 * 16 + (c16 ^ (4 + g4))) * 16;
  if (kvp && w < 4) {
    const int kg = 2 * (j >> 2) + (w >> 1);
    const char* wp = smem + L_WKV + (w & 1) * 16384 + (8 * g4 + (c16 >> 2)) * 32 + (c16 & 3) * 8;
    const char* xp = smem + L_XN + kg * 16384;
    f4 a0 = {0}, a1 = {0};
    u4 wf[16], xf[16];
#pragma unroll
    for (int s = 0; s < 16; ++s) { wf[s] = ldstr2(wp + s * 1024, 128); xf[s] = lds16(xp + s * 1024 + ((s & 1) ? xo1 : xo0)); }
    __builtin_amdgcn_sched_barrier(0);
    if (w & 1) {
#pragma unroll
      for (int s = 0; s < 16; s += 2) { a0 = mfma16(xf[s], wf[s], a0); a1 = mfma16(xf[s + 1], wf[s + 1], a1); }
    } else {
#pragma unroll
      for (int s = 0; s < 16; s += 2) { a0 = mfma16(wf[s], xf[s], a0); a1 = mfma16(wf[s + 1], xf[s + 1], a1); }
    }
    a0 += a1;
    u2 kv = {pk(a0[0], a0[1]), pk(a0[2], a0[3])};
    const int key0 = R0 + 16 * kg;
    if (w & 1) {
      __builtin_amdgcn_raw_buffer_store_b64(kv, rs, WS_V + (key0 >> 4) * 512 + (g4 & 1) * 256 + c16 * 16 + (g4 >> 1) * 8, 0, 16);
      asm volatile("s_waitcnt vmcnt(1)" ::: "memory");
    } else {
      __builtin_amdgcn_raw_buffer_store_b64(kv, rs, WS_K + (key0 >> 5) * 1024 + ((g4 >> 1) * 32 + (key0 & 16) + c16) * 16 + (g4 & 1) * 8, 0, 16);
      float n2 = dot2(kv[0], kv[0], dot2(kv[1], kv[1], 0.f));
      n2 = max16(swap32_add(swap16_add(n2)));
      if (lane == 0) __builtin_amdgcn_raw_buffer_store_b32(__float_as_uint(n2), rs, WS_KMAX + (rb * 4 + kg) * 4, 0, 16);
      asm volatile("s_waitcnt vmcnt(2)" ::: "memory");
    }
  } else {
    drain();
  }
  bar();
  STAMP(3);
  const int rt = w & 1, ha = w >> 1;
  u4 qf;
  {
    const int f = ((c16 >> 3) & 1) | ((g4 & 1) << 1);
    const char* wp = smem + L_WQ + (8 * g4 + (c16 >> 2)) * 128 + ((ha ^ f) * 2) * 16 + (c16 & 3) * 8;
    const char* xp = smem + L_XN + rt * 32768;
    f4 a0 = {0}, a1 = {0};
    u4 wf[16], x0[16], x1[16];
#define Q_READ(s) do { wf[s] = ldstr2(wp + (s) * 4096, 512); x0[s] = lds16(xp + (s) * 1024 + (((s) & 1) ? xo1 : xo0)); \
                       x1[s] = lds16(xp + 16384 + (s) * 1024 + (((s) & 1) ? xo1 : xo0)); } while (0)
    Q_READ(0); Q_READ(1); Q_READ(2);
#pragma unroll
    for (int s = 0; s < 16; ++s) {
      __builtin_amdgcn_sched_barrier(0);
      if (s + 3 < 16) Q_READ(s + 3);
      __builtin_amdgcn_sched_barrier(0);
      a0 = mfma16(wf[s], x0[s], a0);
      a1 = mfma16(wf[s], x1[s], a1);
      if (s == KVS && kvp) {
        drain();
        bar();
        if (tid == 0) put_flag(ws, WS_F1 + (rb * 2 + (j >> 2)) * 128, tag);
      }
    }
    const float qs = 0.25f * LOG2E;
    auto r0 = __builtin_amdgcn_permlane16_swap(pk(a0[0] * qs, a0[1] * qs), pk(a1[0] * qs, a1[1] * qs), false, false);
    auto r1 = __builtin_amdgcn_permlane16_swap(pk(a0[2] * qs, a0[3] * qs), pk(a1[2] * qs, a1[3] * qs), false, false);
    qf = u4{r0[0], r1[0], r0[1], r1[1]};
    *(u4*)(smem + L_QX + w * 1024 + lane * 16) = qf;
  }
  STAMP(4);
  if (w == 0) wait_flags<PSL1>(ws, WS_F1 + lane * 128, tag);
  bar();
  STAMP(5);

  u2 w1f[16][4];
  const __amdgpu_buffer_rsrc_t rw1 = __builtin_amdgcn_make_buffer_rsrc((void*)w1, 0, 512 * 3584, 0x00020000);
  const int w1o = w < 7 ? (lane >> 2) * 3584 + (224 * j + 32 * w) * 2 + ((lane & 3) ^ ((((lane >> 3) ^ (lane >> 4)) & 1) << 1)) * 16 : 0x40000000;
#define W1_DMA(i) __builtin_amdgcn_raw_ptr_buffer_load_lds(rw1, (AS3 void*)(smem + L_T1 + w * 4096 + ((i) & 3) * 1024), 16, w1o, 16 * (i) * 3584, 0, 0)
  const unsigned t0 = (unsigned)(size_t)(AS3 char*)(smem + L_T1 + w * 4096 + (16 * (g4 >> 1) + 4 * (g4 & 1) + (c16 >> 2)) * 64 +
                                                    ((((c16 >> 3) ^ g4) & 1) * 2 + ((c16 >> 1) & 1)) * 16 + (c16 & 1) * 8);
  const unsigned t1 = t0 ^ 32;
#define W1_READ(s, n) asm volatile("s_waitcnt vmcnt(%8)\n\tds_read_b64_tr_b16 %0, %4 offset:%6\n\tds_read_b64_tr_b16 %1, %4 offset:%7\n\t" \
    "ds_read_b64_tr_b16 %2, %5 offset:%6\n\tds_read_b64_tr_b16 %3, %5 offset:%7" \
    : "=&v"(w1f[s][0]), "=&v"(w1f[s][1]), "=&v"(w1f[s][2]), "=&v"(w1f[s][3]) : "v"(t0), "v"(t1), "i"(((s) & 1) * 2048), "i"(((s) & 1) * 2048 + 512), "i"(n))
  f16v o;
  u2 xres0, xres1;
  {
    const int sw = (w >> 2) * 32768 + (w & 3) * 1024, so = sw + lane * 16;
#define KV_LOAD(i) do { DMA_SC1(WS_K + (i) * 4096, so, L_K + sw + (i) * 4096); DMA_SC1(WS_V + (i) * 4096, so, L_V + sw + (i) * 4096); } while (0)
    const u4 qb = lds16(smem + L_QX + (w ^ 1) * 1024 + lane * 16);
    asm volatile("" ::: "memory");
    KV_LOAD(0);
    const u2 km = __builtin_bit_cast(u2, __builtin_amdgcn_raw_buffer_load_b64(rs, WS_KMAX + lane * 8, 0, 16));
    const float qn = fmaxf(swap32_add(dot2(qf[0], qf[0], dot2(qf[1], qf[1], dot2(qf[2], qf[2], dot2(qf[3], qf[3], 0.f))))),
                           swap32_add(dot2(qb[0], qb[0], dot2(qb[1], qb[1], dot2(qb[2], qb[2], dot2(qb[3], qb[3], 0.f))))));
    float kn = fmaxf(__uint_as_float(km[0]), __uint_as_float(km[1]));
    kn = swap32_max(swap16_max(max16(kn)));
    const bool safe = __all(qn * kn <= SAFE_BOUND * SAFE_BOUND);
    drain();
    bar();
    STAMP(6);
    const bool vreal = r32 < 16;
    const unsigned vc = (r32 == 16 || r32 == 20) ? 0x3f803f80u : 0u;
    const unsigned ka = (unsigned)(size_t)(AS3 char*)(smem + L_K + rt * 32768 + lane * 16);
    const unsigned va = (unsigned)(size_t)(AS3 char*)(smem + L_V + rt * 32768 + h2 * 256 + c16 * 16);
    const u64 vm = 0x0000ffff0000ffffull;
    u4 kf, v0 = {vc, vc, vc, vc}, v1 = v0, p0, p1 = {0, 0, 0, 0};
#define K_READ(u) asm volatile("ds_read_b128 %0, %1 offset:%2" : "=v"(kf) : "v"(ka), "i"((u) * 1024))
#define V_READ(v, u, hf) asm volatile("s_mov_b64 exec, %3\n\tds_read_b128 %0, %1 offset:%2\n\ts_mov_b64 exec, -1" : "+v"(v) : "v"(va), "i"((u) * 1024 + (hf) * 512), "s"(vm))
#define LGKM(n, v) asm volatile("s_waitcnt lgkmcnt(" #n ")")
#define SB() __builtin_amdgcn_sched_barrier(0)
#define EXPS(s, a, b) _Pragma("unroll") for (int i = a; i < b; ++i) asm volatile("v_exp_f32 %0, %0" : "+v"(s[i]))
#define PKS(s, b) u4{pk(s[b], s[b + 1]), pk(s[b + 2], s[b + 3]), pk(s[b + 4], s[b + 5]), pk(s[b + 6], s[b + 7])}
    const f16v zero = {0};
    f16v oa = zero, ob = zero, sa, sb;
    unsigned pf0 = 0;
    const unsigned pfo = (w * 8 + (lane & 7)) * 1024;
#define PV(o, v, p) asm volatile("v_mfma_f32_32x32x16_bf16 %0, %1, %2, %0" : "+v"(o) : "v"(v), "v"(p))
#define QK(s, q) asm volatile("v_mfma_f32_32x32x16_bf16 %0, %1, %2, 0" : "=&v"(s) : "v"(kf), "v"(q))
    W1_DMA(0); W1_DMA(1);
    if (w >= 4) __builtin_amdgcn_s_setprio(1);
    K_READ(0); V_READ(v0, 0, 0);
    LGKM(1, kf);
    QK(sa, qf);
    SB();
    asm volatile("s_nop 7\n\ts_nop 7\n\ts_nop 7" : "+v"(sa), "+v"(oa), "+v"(ob), "+v"(p1), "+v"(v1));
#pragma unroll
    for (int u = 0; u < 32; ++u) {
      SB();
      if (u == 3) asm volatile("s_waitcnt vmcnt(11)" ::: "memory");
      if (u == 8) asm volatile("s_waitcnt vmcnt(%0)" :: "i"(5 + (PFS <= 5) + (PFS <= 7)) : "memory");
      if (u == 3 || u == 8) bar();
      if (u < 4) { KV_LOAD(2 * u + 1); if (u < 3) KV_LOAD(2 * u + 2); }
      if (u < 30) W1_DMA(u + 2);
      SB();
      EXPS(sa, 0, 2); SB();
      PV(ob, v1, p1); SB();
      V_READ(v1, u, 1);
      EXPS(sa, 2, 6); SB();
      QK(sb, qb); SB();
      if (u < 31) K_READ(u + 1); else K_READ(u);
      if (u & 1) W1_READ(u >> 1, u == 1 ? 10 : u == 3 ? 8 : u == 31 ? 0 : 2 + (u == 11 ? 2 : 0) + ((u >= PFS + 2 && u <= PFS + 18) ? 1 : 0));
      EXPS(sa, 6, 8); SB();
      p0 = PKS(sa, 0); SB();
      EXPS(sa, 8, 10);
      if (u & 1) LGKM(6, v0); else LGKM(2, v0);
      SB();
      PV(oa, v0, p0); SB();
      EXPS(sa, 10, 16); SB();
      p1 = PKS(sa, 8); SB();
      if ((u & 1) && u >= PFS && u <= PFS + 16) {
        const int ix = (u - PFS) >> 1, cl = (bid >> 3) & 3;
        const unsigned char* pb = (ix < 7 ? w2 + (size_t)(cl * 448 + ix * 64) * 1024 : wo + (size_t)(cl * 128 + (ix - 7) * 64) * 1024) + j * 128;
        asm volatile("s_mov_b64 exec, 0xff\n\tglobal_load_dword %0, %1, %2\n\ts_mov_b64 exec, -1" : "+v"(pf0) : "v"(pfo), "s"(pb));
      }
      if (u == 9) {
        const unsigned char* xr = x + (size_t)(R0 + 32 * rt + r32) * 1024 + (64 * j + 16 * ha + 4 * h2) * 2;
        xres0 = *(const u2*)xr; xres1 = *(const u2*)(xr + 16);
      }
      if (u == 31) asm volatile("" :: "v"(pf0));

      SB();
      EXPS(sb, 0, 2);
      if (u & 1) LGKM(5, v1); else LGKM(1, v1);
      SB();
      PV(oa, v1, p1); SB();
      EXPS(sb, 2, 6);
      LGKM(0, kf);
      if (u & 1) asm volatile("" : "+v"(w1f[u >> 1][0]), "+v"(w1f[u >> 1][1]), "+v"(w1f[u >> 1][2]), "+v"(w1f[u >> 1][3]));
      SB();
      QK(sa, qf); SB();
      EXPS(sb, 6, 8); SB();
      p0 = PKS(sb, 0); SB();
      EXPS(sb, 8, 10); SB();
      PV(ob, v0, p0); SB();
      if (u < 31) V_READ(v0, u + 1, 0);
      EXPS(sb, 10, 16); SB();
      p1 = PKS(sb, 8);
    }
    __builtin_amdgcn_s_setprio(0);
    asm volatile("s_nop 1");
    PV(ob, v1, p1);
    asm volatile("s_nop 7\n\ts_nop 7\n\ts_nop 7" : "+v"(oa), "+v"(ob));
    SB();
    char* xo = smem + L_T1 + w * 4096 + lane * 16;
    *(f4*)xo = f4{ob[0], ob[1], ob[2], ob[3]};
    *(f4*)(xo + 1024) = f4{ob[4], ob[5], ob[6], ob[7]};
    *(float*)(smem + L_T1 + w * 4096 + 2048 + lane * 4) = ob[8];
    if (!safe) {
      float m = -3.0e38f;
      oa = zero;
      const char* kp = smem + L_K + lane * 16;
      const char* vp = smem + L_V + h2 * 256 + c16 * 16;
#pragma unroll 1
      for (int t = 0; t < 64; ++t) tile_slow(kp + t * 1024, vp + t * 1024, vreal, vc, qf, m, oa);
    }
    bar();
    o = oa;
    if (safe) {
      const char* xi = smem + L_T1 + (w ^ 1) * 4096 + lane * 16;
      const f4 a = *(const f4*)xi, b = *(const f4*)(xi + 1024);
#pragma unroll
      for (int e = 0; e < 4; ++e) { o[e] += a[e]; o[4 + e] += b[e]; }
      o[8] += *(const float*)(smem + L_T1 + (w ^ 1) * 4096 + 2048 + lane * 4);
    }
  }
  STAMP(7);
  {
  int tid = threadIdx.x;
  asm volatile("" : "+v"(tid));
  const int lane = tid & 63, c16 = lane & 15, g4 = lane >> 4, h2 = lane >> 5;
  {
    const float il = __builtin_amdgcn_rcpf(o[8]);
    u4 of = {pk(o[0] * il, o[1] * il), pk(o[2] * il, o[3] * il), pk(o[4] * il, o[5] * il), pk(o[6] * il, o[7] * il)};
    ST_SC1(of, WS_O + ((rb * 2 + rt) * 32 + 4 * j + ha) * 1024, lane * 16);
  }
  asm volatile("" ::: "memory");
  const unsigned w2v = (lane >> 3) * 1024 + ((lane & 7) ^ (((lane >> 4) & 1) << 2)) * 16;
#define W_DMA(src, dst) V_DMA(w2v, src, dst)
#pragma unroll
  for (int i = 0; i < 8; ++i) W_DMA(wo + (size_t)(i * 64 + w * 8) * 1024 + j * 128, L_WO + (i * 8 + w) * 1024);
  asm volatile("s_waitcnt vmcnt(8)" ::: "memory");
  bar();
  if (tid == 0) put_flag(ws, WS_F2 + (rb * 8 + j) * 128, tag);
  STAMP(8);

  u4 a1f[16][2];
  f4 cs0 = {0}, cs1 = {0};
  const u4 ones = {0x3f803f80u, 0x3f803f80u, 0x3f803f80u, 0x3f803f80u};
#pragma unroll
  for (int s = 0; s < 16; ++s) {
    a1f[s][0] = u4{w1f[s][0][0], w1f[s][0][1], w1f[s][1][0], w1f[s][1][1]};
    a1f[s][1] = u4{w1f[s][2][0], w1f[s][2][1], w1f[s][3][0], w1f[s][3][1]};
    cs0 = mfma16(a1f[s][0], ones, cs0); cs1 = mfma16(a1f[s][1], ones, cs1);
  }
  const unsigned char* bp = b1 + (224 * j + 32 * (w < 7 ? w : 6) + 4 * g4) * 2;
  const u2 ba = *(const u2*)bp, bb = *(const u2*)(bp + 32);
  if (tid == 0) *(volatile AS3 unsigned*)(AS3 char*)(smem + L_SF) = 0;
  if (c16 == 0) {
    char* cp = smem + L_CS + (w * 4 + g4) * 64;
    *(f4*)cp = cs0; *(f4*)(cp + 16) = cs1;
    *(f4*)(cp + 32) = f4{lo16(ba[0]), hi16(ba[0]), lo16(ba[1]), hi16(ba[1])};
    *(f4*)(cp + 48) = f4{lo16(bb[0]), hi16(bb[0]), lo16(bb[1]), hi16(bb[1])};
  }
  const unsigned char* w2p = w2 + (size_t)(224 * w) * 1024 + j * 128;
#define W2_DMA(s, dst) _Pragma("unroll") for (int q = 0; q < 4; ++q) W_DMA(w2p + (size_t)(32 * (s) + 8 * q) * 1024, (dst) + q * 1024)
  const int kq = ha;
  wait_flags<PSL>(ws, WS_F2 + (rb * 8 + 2 * kq + (lane & 1)) * 128, tag);
  STAMP(9);
  float hv[8];
  {
    u4 ob[8];
#pragma unroll
    for (int i = 0; i < 8; ++i) { ob[i] = LD_SC1(WS_O + ((rb * 2 + rt) * 32 + 8 * kq + i) * 1024, lane * 16); SB(); }
    asm volatile("s_waitcnt vmcnt(8)" ::: "memory");
    bar();
    const int q = c16 >> 2, u = g4 & 1;
    const char* wp = smem + L_WO + (kq * 128 + 4 * h2 + q) * 128 + (c16 & 3) * 8;
    const int po0 = (u ^ ((q >> 1) << 1)) * 32, po1 = ((2 + u) ^ ((q >> 1) << 1)) * 32;
    f16v acc[2] = {{0}, {0}};
#pragma unroll
    for (int i = 0; i < 8; ++i) {
      acc[0] = mfma32(ldstr2(wp + i * 2048 + po0, 1024), ob[i], acc[0]);
      acc[1] = mfma32(ldstr2(wp + i * 2048 + po1, 1024), ob[i], acc[1]);
    }
    STAMP(10);
    if (!D4 && w < 7) { W2_DMA(4, L_T1 + w * 4096); }
#pragma unroll
    for (int d = 0; d < 4; ++d) {
      if (d != kq) {
        char* rp = smem + L_RED1 + ((rt + 2 * d) * 4 + kq) * 2048 + lane * 16;
        const f16v& a = acc[d >> 1];
        const int b = 8 * (d & 1);
        *(f4*)rp = f4{a[b], a[b + 1], a[b + 2], a[b + 3]};
        *(f4*)(rp + 1024) = f4{a[b + 4], a[b + 5], a[b + 6], a[b + 7]};
      } else {
#pragma unroll
        for (int e = 0; e < 8; ++e) hv[e] = acc[d >> 1][8 * (d & 1) + e];
      }
    }
    bar();
#pragma unroll
    for (int d = 1; d < 4; ++d) {
      const char* rp = smem + L_RED1 + (w * 4 + ((kq + d) & 3)) * 2048 + lane * 16;
      f4 a = *(const f4*)rp, b = *(const f4*)(rp + 1024);
#pragma unroll
      for (int e = 0; e < 4; ++e) { hv[e] += a[e]; hv[4 + e] += b[e]; }
    }
    u4 hf = {pk(hv[0] + lo16(xres0[0]), hv[1] + hi16(xres0[0])), pk(hv[2] + lo16(xres0[1]), hv[3] + hi16(xres0[1])),
             pk(hv[4] + lo16(xres1[0]), hv[5] + hi16(xres1[0])), pk(hv[6] + lo16(xres1[1]), hv[7] + hi16(xres1[1]))};
    ST_SC1(hf, WS_H + ((rb * 2 + rt) * 32 + 4 * j + kq) * 1024, lane * 16);
  }
  drain();
  bar();
  if (tid == 0) put_flag(ws, WS_F3 + (rb * 8 + j) * 128, tag);
  STAMP(11);

  if (EARLY > 0) { W2_DMA(0, L_W2A + w * 8192); }
  if (EARLY > 1) { W2_DMA(1, L_W2A + w * 8192 + 4096); }
  wait_flags<PSL>(ws, WS_F3 + (rb * 8 + w) * 128, tag);
  STAMP(12);
  {
#pragma unroll
    for (int i = 0; i < 8; ++i)
      DMA_SC1(WS_H + ((rb * 2 + (i >> 2)) * 32 + 4 * w + (i & 3)) * 1024, lane * 16, L_H + ((i >> 2) * 32 + 4 * w + (i & 3)) * 1024);
    drain();
  }
  bar();
  STAMP(13);
  u4 af[7][4];
  const int ro = (4 * g4 + (c16 >> 2)) * 128, co0 = ((c16 & 3) ^ (((c16 >> 3) & 1) << 2)) * 16, co1 = co0 ^ 64;
#define W2_FRAG(base, t) ldstr2(smem + (base) + ro + (((t) >> 1) ? co1 : co0) + ((t) & 1) * 8, 2048)
#define W2_REGS() drain(); _Pragma("unroll") for (int t = 0; t < 4; ++t) { af[0][t] = W2_FRAG(L_W2A + w * 8192, t); af[1][t] = W2_FRAG(L_W2A + w * 8192 + 4096, t); }
#define W2_NEXT() do { asm volatile("s_waitcnt lgkmcnt(0)" ::: "memory"); W2_DMA(5, L_W2A + w * 8192); W2_DMA(6, L_W2A + w * 8192 + 4096); } while (0)
  u2 hr0, hr1;
  {
    const int mt = w >> 1;
    const char* hp = smem + L_H + ((mt >> 1) * 32 + 4 * j + 2 * (w & 1) + (g4 >> 1)) * 1024 + (16 * (mt & 1) + c16) * 16 + (g4 & 1) * 8;
    hr0 = *(const u2*)hp; hr1 = *(const u2*)(hp + 512);
  }
  {
    const char* hp = smem + L_H + (g4 >> 1) * 1024 + ((g4 & 1) * 32 + c16) * 16;
#define H_READ(s, mt) lds16(hp + ((mt) >> 1) * 32768 + (s) * 2048 + ((mt) & 1) * 256)
    if (w == 7) {
      f4 s1[4] = {}, s2[4] = {};
      u4 hb[2][4];
      __builtin_amdgcn_s_setprio(2);
#pragma unroll
      for (int mt = 0; mt < 4; ++mt) hb[0][mt] = H_READ(0, mt);
#pragma unroll
      for (int s = 0; s < 16; ++s) {
        if (s < 15) {
#pragma unroll
          for (int mt = 0; mt < 4; ++mt) hb[(s + 1) & 1][mt] = H_READ(s + 1, mt);
        }
        if (s == 0 && EARLY < 1) { W2_DMA(0, L_W2A + w * 8192); }
        if (s == 2 && EARLY < 2) { W2_DMA(1, L_W2A + w * 8192 + 4096); }
        SB();
#pragma unroll
        for (int mt = 0; mt < 4; ++mt) { s1[mt] = mfma16(ones, hb[s & 1][mt], s1[mt]); s2[mt] = mfma16(hb[s & 1][mt], hb[s & 1][mt], s2[mt]); }
        SB();
      }
#pragma unroll
      for (int mt = 0; mt < 4; ++mt) {
        const float mean = s1[mt][0] * (1.f / 512.f);
        const float sq = (c16 & 2) ? ((c16 & 1) ? s2[mt][3] : s2[mt][2]) : ((c16 & 1) ? s2[mt][1] : s2[mt][0]);
        const float rstd = __builtin_amdgcn_rsqf(fmaxf(sq * (1.f / 512.f) - mean * mean, 0.f) + 1e-5f);
        if (g4 == (c16 >> 2)) *(f2*)(smem + L_ST + (16 * mt + c16) * 8) = f2{mean, rstd};
      }
      asm volatile("s_waitcnt lgkmcnt(0)" ::: "memory");
      if (lane == 0) *(volatile AS3 unsigned*)(AS3 char*)(smem + L_SF) = 1;
      W2_REGS();
      W2_NEXT();
      if (bid == 0) {
#pragma unroll 1
        for (int g = 0; g < 4; ++g) wait_flags<PSL>(ws, WS_F2 + (g * 64 + lane) * 128, tag);
      }
    } else {
      f4 acc[2][4] = {};
      u4 hb[2][4];
#pragma unroll
      for (int mt = 0; mt < 4; ++mt) hb[0][mt] = H_READ(0, mt);
#pragma unroll
      for (int s = 0; s < 16; ++s) {
        if (s < 15) {
#pragma unroll
          for (int mt = 0; mt < 4; ++mt) hb[(s + 1) & 1][mt] = H_READ(s + 1, mt);
        }
        if (s == 0 && EARLY < 1) { W2_DMA(0, L_W2A + w * 8192); }
        if (s == 2 && EARLY < 2) { W2_DMA(1, L_W2A + w * 8192 + 4096); }
        if (D4 && s == 4) { W2_DMA(4, L_T1 + w * 4096); }
        if (s == SR) { W2_REGS(); }
        if (s == SR + 2) { asm volatile("s_waitcnt lgkmcnt(0)" ::: "memory"); W2_DMA(5, L_W2A + w * 8192); }
        if (s == SR + 4) { W2_DMA(6, L_W2A + w * 8192 + 4096); }
        SB();
#pragma unroll
        for (int mt = 0; mt < 4; ++mt) {
          acc[0][mt] = mfma16(a1f[s][0], hb[s & 1][mt], acc[0][mt]);
          acc[1][mt] = mfma16(a1f[s][1], hb[s & 1][mt], acc[1][mt]);
        }
        SB();
      }
      STAMP(14);
      while (*(volatile AS3 unsigned*)(AS3 char*)(smem + L_SF) == 0) __builtin_amdgcn_s_sleep(1);
      asm volatile("" ::: "memory");
      const char* cp = smem + L_CS + (w * 4 + g4) * 64;
      const f4 c0 = *(const f4*)cp, c1 = *(const f4*)(cp + 16), bb0 = *(const f4*)(cp + 32), bb1 = *(const f4*)(cp + 48);
      f2 stv[4];
#pragma unroll
      for (int mt = 0; mt < 4; ++mt) stv[mt] = *(const f2*)(smem + L_ST + (16 * mt + c16) * 8);
#pragma unroll
      for (int mt = 0; mt < 4; ++mt) {
        const f2 st = stv[mt];
        float y[8];
#pragma unroll
        for (int e = 0; e < 8; ++e) {
          const float cs = (e >> 2) ? c1[e & 3] : c0[e & 3];
          const float bb = (e >> 2) ? bb1[e & 3] : bb0[e & 3];
          const float v = fmaf(st[1], fmaf(-st[0], cs, acc[e >> 2][mt][e & 3]), bb);
          const float ex = __builtin_amdgcn_exp2f(v * LOG2E);
          const float r = __builtin_amdgcn_rcpf(fmaf(ex, ex + 2.f, 2.f));
          y[e] = fmaf(-2.f, v * r, v);
        }
        ST_SC1((u4{pk(y[0], y[1]), pk(y[2], y[3]), pk(y[4], y[5]), pk(y[6], y[7])}), WS_ACT + ((rb * 4 + mt) * 56 + 7 * j + w) * 1024, lane * 16);
      }
    }
  }
  drain();
  bar();
  __builtin_amdgcn_s_setprio(0);
  if (tid == 0) put_flag(ws, WS_F4 + (rb * 8 + j) * 128, tag);
  STAMP(15);

  {
    W2_DMA(2, L_H + w * 8192); W2_DMA(3, L_H + w * 8192 + 4096);
    if (w == 7) { W2_DMA(4, L_T1 + w * 4096); }
    wait_flags<PSL>(ws, WS_F4 + (rb * 8 + w) * 128, tag);
    STAMP(16);
    f4 acc[4][4] = {};
    u4 ab[7][4];
#define ACT_LOAD(s) _Pragma("unroll") for (int mt = 0; mt < 4; ++mt) ab[s][mt] = LD_SC1(WS_ACT + ((rb * 4 + mt) * 56 + 7 * w + (s)) * 1024, lane * 16)
    drain();
    asm volatile("s_waitcnt lgkmcnt(0)" : "+v"(af[0][0]), "+v"(af[0][1]), "+v"(af[0][2]), "+v"(af[0][3]), "+v"(af[1][0]), "+v"(af[1][1]), "+v"(af[1][2]), "+v"(af[1][3]));
    SB(); ACT_LOAD(0); SB(); ACT_LOAD(1); SB(); ACT_LOAD(2); SB();
    if (AD > 3) { ACT_LOAD(3); }
#pragma unroll
    for (int s = 0; s < 7; ++s) {
      SB();
      if (s >= 1 && s < 6) {
        const int base = (s == 1 ? L_H : s == 2 ? L_H + 4096 : s == 4 ? L_W2A : L_W2A + 4096) + w * 8192;
#pragma unroll
        for (int t = 0; t < 4; ++t) af[s + 1][t] = W2_FRAG(s == 3 ? L_T1 + w * 4096 : base, t);
      }
      SB();
#pragma unroll
      for (int mt = 0; mt < 4; ++mt)
#pragma unroll
        for (int t = 0; t < 4; ++t) acc[t][mt] = mfma16(af[s][t], ab[s][mt], acc[t][mt]);
      SB();
      if (s + AD < 7) { ACT_LOAD(s + AD); }
    }
    STAMP(17);
    float y[8] = {};
#pragma unroll
    for (int d = 0; d < 8; ++d) {
      const f4 a0 = acc[2 * (d & 1)][d >> 1], a1 = acc[2 * (d & 1) + 1][d >> 1];
      *(u4*)(smem + L_RED2 + (w * 8 + d) * 1024 + lane * 16) = u4{pk(a0[0], a0[1]), pk(a0[2], a0[3]), pk(a1[0], a1[1]), pk(a1[2], a1[3])};
    }
    bar();
#pragma unroll
    for (int d = 0; d < 8; ++d) {
      const u4 a = lds16(smem + L_RED2 + (d * 8 + w) * 1024 + lane * 16);
#pragma unroll
      for (int e = 0; e < 4; ++e) { y[2 * e] += lo16(a[e]); y[2 * e + 1] += hi16(a[e]); }
    }
    u4 r = {pk(y[0] + lo16(hr0[0]), y[1] + hi16(hr0[0])), pk(y[2] + lo16(hr0[1]), y[3] + hi16(hr0[1])),
            pk(y[4] + lo16(hr1[0]), y[5] + hi16(hr1[0])), pk(y[6] + lo16(hr1[1]), y[7] + hi16(hr1[1]))};
    const __amdgpu_buffer_rsrc_t ro_ = __builtin_amdgcn_make_buffer_rsrc((void*)out, 0, 2048 * 1024, 0x00020000);
    __builtin_amdgcn_raw_buffer_store_b128(r, ro_, (R0 + 16 * (w >> 1) + c16) * 1024 + (64 * j + 32 * (w & 1) + 8 * g4) * 2, 0, 0);
  }
  if (bid == 0 && tid == 0) put_flag(ws, WS_E, tag);
  STAMP(18);
  }
}

extern "C" void kernel_launch(void* const* d_in, const int* in_sizes, int n_in, void* d_out, int out_size, void* d_ws,
                              size_t ws_size, hipStream_t stream) {
  typedef const unsigned char* cp;
  fused_block_kernel<<<256, 512, 0, stream>>>((cp)d_in[0], (cp)d_in[1], (cp)d_in[2], (cp)d_in[3], (cp)d_in[4], (cp)d_in[6],
                                              (cp)d_in[7], (cp)d_in[8], (unsigned char*)d_out, (unsigned char*)d_ws);
}
```
